```python
import torch

def _softmax(x, axis=-1):
    return torch.softmax(x, dim=axis)
def _gelu(x):
    return torch.nn.functional.gelu(x, approximate="tanh")
def _silu(x):
    return torch.nn.functional.silu(x)
def _sigmoid(x):
    return torch.sigmoid(x)
def _layernorm(x, axis=-1, eps=1e-5):
    m = x.mean(dim=axis, keepdim=True)
    v = x.var(dim=axis, keepdim=True, unbiased=False)
    return (x - m) / torch.sqrt(v + eps)
def _scatter_add(tgt, idx, src, axis=0):
    return tgt.clone().index_add_(axis, idx, src)
def _segment_sum(src, seg, n):
    out = torch.zeros((n,) + src.shape[1:], dtype=src.dtype)
    return out.index_add_(0, seg, src)
def _pad(x, pw):
    flat = tuple(v for pair in reversed(pw) for v in pair)
    return torch.nn.functional.pad(x, flat)

kernel_name = 'standalone__self_sup_conv_stem_wavlm__speech'

def setup_inputs():

    _w = (lambda a: (lambda t: t.to(torch.bfloat16) if t.is_floating_point() else t)(torch.as_tensor(a)))
    B, T, D, K = 2, 16000, 512, 3
    scale = (K * D) ** -0.5
    return {
        "x": _w((lambda *s: torch.randn(*s, dtype=torch.bfloat16) * 0.05)(B, T, D)),
        "w": _w((lambda *s: torch.randn(*s, dtype=torch.bfloat16) * 0.05)(K, D, D) * scale),
        "gn_gamma": _w((lambda *s: torch.randn(*s, dtype=torch.bfloat16) * 0.05)(D) * 0.1 + 1.0),
        "gn_beta": _w((lambda *s: torch.randn(*s, dtype=torch.bfloat16) * 0.05)(D) * 0.1),
    }

def reference(x, w, gn_gamma, gn_beta):
    B, T, D = x.shape
    K = w.shape[0]
    s = 2
    L = (T - K) // s + 1
    h = torch.einsum('blc,cd->bld', x[:, 0:s * L:s, :], w[0])
    for i in range(1, K):
        h = h + torch.einsum('blc,cd->bld', x[:, i:i + s * L:s, :], w[i])
    mu = torch.einsum('bld->bd', h)[:, None, :] / L
    var = torch.einsum('bld->bd', (h - mu) ** 2)[:, None, :] / L
    h = (h - mu) / torch.sqrt(var + 1e-5)
    h = h * gn_gamma[None, None, :] + gn_beta[None, None, :]
    return _gelu(h)
```

```cpp
#include <hip/hip_runtime.h>
#include <hip/hip_bf16.h>
typedef unsigned short u16;
__device__ inline float bf2f(u16 v){ return __uint_as_float(((unsigned)v)<<16); }
__global__ void k_conv(const u16* x, const u16* w, float* h){
  int l0 = blockIdx.x*8; int b = blockIdx.y; int d = threadIdx.x;
  __shared__ float xs[17*512];
  for(int i=threadIdx.x;i<17*512;i+=512){ int t=2*l0+i/512; xs[i] = t<16000 ? bf2f(x[((size_t)b*16000+t)*512 + (i%512)]) : 0.f; }
  __syncthreads();
  float acc[8]={0,0,0,0,0,0,0,0};
  for(int k=0;k<1536;k++){ float wv=bf2f(w[(size_t)k*512+d]);
    #pragma unroll
    for(int r=0;r<8;r++) acc[r]+=xs[r*1024+k]*wv; }
  for(int r=0;r<8;r++){ int l=l0+r; if(l<7999) h[((size_t)b*7999+l)*512+d]=acc[r]; }
}
__global__ void k_stats(const float* h, float* st){
  int b=blockIdx.y; int d=blockIdx.x*64+(threadIdx.x&63); int g=threadIdx.x>>6;
  __shared__ double s1[16][64], s2[16][64];
  double s=0,q=0;
  for(int l=g;l<7999;l+=16){ double v=h[((size_t)b*7999+l)*512+d]; s+=v; q+=v*v; }
  s1[g][threadIdx.x&63]=s; s2[g][threadIdx.x&63]=q; __syncthreads();
  if(g==0){ for(int i=1;i<16;i++){ s+=s1[i][threadIdx.x]; q+=s2[i][threadIdx.x]; }
    double m=s/7999.0, v=q/7999.0-m*m; st[(b*512+d)*2]=(float)m; st[(b*512+d)*2+1]=(float)(1.0/sqrt(v+1e-5)); }
}
__global__ void k_norm(const float* h, const float* st, const u16* gam, const u16* bet, __hip_bfloat16* out){
  size_t i=(size_t)blockIdx.x*256+threadIdx.x; if(i>=(size_t)2*7999*512) return;
  int d=i%512; int b=i/(7999*512);
  float y=(h[i]-st[(b*512+d)*2])*st[(b*512+d)*2+1]*bf2f(gam[d])+bf2f(bet[d]);
  float u=0.7978845608f*(y+0.044715f*y*y*y);
  out[i]=__float2bfloat16(0.5f*y*(1.f+tanhf(u)));
}
extern "C" void kernel_launch(void* const* d_in, const int* in_sizes, int n_in, void* d_out, int out_size, void* d_ws, size_t ws_size, hipStream_t stream){
  const u16* x=(const u16*)d_in[0]; const u16* w=(const u16*)d_in[1];
  float* h=(float*)d_ws; float* st=h+(size_t)2*8000*512;
  k_conv<<<dim3(1000,2),512,0,stream>>>(x,w,h);
  k_stats<<<dim3(8,2),1024,0,stream>>>(h,st);
  k_norm<<<(2*7999*512+255)/256,256,0,stream>>>(h,st,(const u16*)d_in[2],(const u16*)d_in[3],(__hip_bfloat16*)d_out);
}
```

```cpp
#include <hip/hip_runtime.h>

typedef unsigned u32x4 __attribute__((ext_vector_type(4)));
typedef unsigned long long u64;
#define LAS __attribute__((address_space(3)))


struct ArgLine { const void* p[6]; u64 zero[2]; };
struct Args { ArgLine l[64]; };

__global__ __launch_bounds__(256, 1) void conv_stem_kernel(Args) {
  __shared__ __attribute__((aligned(16))) unsigned char lds[163840];
  unsigned tid = threadIdx.x, bid = blockIdx.x; u64 kp = (u64)__builtin_amdgcn_kernarg_segment_ptr();
  asm volatile(
    "s_lshl_b32 s3, s2, 4\n\t"
    "s_andn2_b32 s3, s3, 63\n\t"
    "s_add_u32 s0, s0, s3\n\t"
    "s_addc_u32 s1, s1, 0\n\t"
    "s_load_dwordx8 s[8:15], s[0:1], 0x0\n\t"
    "s_load_dwordx4 s[16:19], s[0:1], 0x20\n\t"
    "s_getpc_b64 s[4:5]\n\t"
    "L_pf_base:\n\t"
    "v_lshrrev_b32 v56, 6, v0\n\t"
    "v_and_b32 v1, 1, v0\n\t"
    "v_readfirstlane_b32 s33, v56\n\t"
    "v_lshl_or_b32 v56, v56, 1, v1\n\t"
    "s_andn2_b32 s3, s2, 7\n\t"
    "v_add_lshl_u32 v56, v56, s3, 7\n\t"
    "v_min_u32 v56, L_kernel_end_marker-L_pf_base, v56\n\t"
    "global_load_dword v56, v56, s[4:5]\n\t"
    "s_and_b32 s56, s2, 7\n\t"
    "s_lshr_b32 s3, s2, 5\n\t"
    "s_lshl3_add_u32 s59, s56, s3\n\t"
    "s_mul_i32 s20, s59, 512000\n\t"
    "s_and_b32 s57, s59, 31\n\t"
    "s_cmp_eq_u32 s57, 31\n\t"
    "s_cselect_b32 s21, 0x800, 0\n\t"
    "s_sub_u32 s42, 513024, s21\n\t"
    "s_mov_b32 s43, 0x20000\n\t"
    "s_lshl_b32 s64, s56, 7\n\t"
    "s_lshl_b32 s26, s33, 13\n\t"
    "s_add_u32 s26, s26, 0x10000\n\t"
    "s_mov_b32 m0, s26\n\t"
    "s_lshr_b32 s80, s33, 1\n\t"
    "s_and_b32 s81, s33, 1\n\t"
    "s_lshl_b32 s3, s80, 5\n\t"
    "s_add_u32 s3, s3, s81\n\t"
    "s_lshl_b32 s3, s3, 2\n\t"
    "v_mov_b32 v61, v0\n\t"
    "v_lshrrev_b32 v1, 3, v0\n\t"
    "v_and_b32 v2, 7, v0\n\t"
    "v_and_b32 v1, 6, v1\n\t"
    "v_xor_b32 v1, v1, v2\n\t"
    "v_and_b32 v2, 56, v0\n\t"
    "v_add_lshl_u32 v2, v2, s3, 11\n\t"
    "v_lshl_or_b32 v0, v1, 4, v2\n\t"
    "v_add_u32 v1, 130048, v0\n\t"
    "v_add_u32 v2, 0x1000, v0\n\t"
    "v_add_u32 v3, 0x1000, v1\n\t"
    "s_waitcnt lgkmcnt(0)\n\t"
    "s_add_u32 s40, s8, s20\n\t"
    "s_addc_u32 s41, s9, 0\n\t"
    "buffer_load_dwordx4 v0, s[40:43], s64 offen offset:0 lds\n\t"
    "s_bfe_u32 s82, s2, 0x20003\n\t"
    "s_lshl_b32 s23, s82, 8\n\t"
    "s_lshl_b32 s3, s80, 6\n\t"
    "buffer_load_dwordx4 v1, s[40:43], s64 offen offset:1024 lds\n\t"
    "s_add_u32 s3, s3, s81\n\t"
    "s_lshl_b32 s3, s3, 14\n\t"
    "s_lshl_b32 s72, s56, 16\n\t"
    "buffer_load_dwordx4 v0, s[40:43], s64 offen offset:2048 lds\n\t"
    "s_add_u32 s48, s10, s23\n\t"
    "s_addc_u32 s49, s11, 0\n\t"
    "s_sub_u32 s50, 0x180000, s23\n\t"
    "buffer_load_dwordx4 v1, s[40:43], s64 offen offset:3072 lds\n\t"
    "s_add_u32 m0, s26, 4096\n\t"
    "s_mov_b32 s51, 0x20000\n\t"
    "s_lshl_b32 s27, s33, 12\n\t"
    "v_bfe_u32 v4, v61, 4, 2\n\t"
    "buffer_load_dwordx4 v2, s[40:43], s64 offen offset:0 lds\n\t"
    "v_and_b32 v5, 15, v61\n\t"
    "v_lshlrev_b32 v6, 1, v4\n\t"
    "v_xor_b32 v6, v5, v6\n\t"
    "buffer_load_dwordx4 v3, s[40:43], s64 offen offset:1024 lds\n\t"
    "v_lshl_add_u32 v7, v4, 10, s3\n\t"
    "v_lshl_or_b32 v8, v6, 4, v7\n\t"
    "v_add_u32 v9, 0xc00, v8\n\t"
    "buffer_load_dwordx4 v2, s[40:43], s64 offen offset:2048 lds\n\t"
    "v_xor_b32 v10, 0x80, v8\n\t"
    "v_add_u32 v11, 0x2400, v10\n\t"
    "v_add_u32 v10, 0x1800, v10\n\t"
    "buffer_load_dwordx4 v3, s[40:43], s64 offen offset:3072 lds\n\t"
    "s_add_u32 m0, s27, 0\n\t"
    "s_nop 0\n\t"
    "buffer_load_dwordx4 v8, s[48:51], s72 offen offset:0 lds\n\t"
    "s_add_u32 s52, s48, 0x8000\n\t"
    "s_addc_u32 s53, s49, 0\n\t"
    "s_add_u32 s65, s64, 0x80\n\t"
    "s_and_b32 s65, s65, 0x3ff\n\t"
    "s_lshl_b32 s73, s65, 9\n\t"
    "s_sub_u32 s54, 0x178000, s23\n\t"
    "buffer_load_dwordx4 v9, s[48:51], s72 offen offset:1024 lds\n\t"
    "s_mov_b32 s55, 0x20000\n\t"
    "s_lshl_b32 s3, s80, 14\n\t"
    "s_add_u32 s3, s3, 0x10000\n\t"
    "v_and_b32 v6, 6, v5\n\t"
    "v_xor_b32 v6, v6, v4\n\t"
    "v_lshl_add_u32 v7, v5, 7, s3\n\t"
    "buffer_load_dwordx4 v10, s[48:51], s72 offen offset:2048 lds\n\t"
    "v_lshl_or_b32 v16, v6, 4, v7\n\t"
    "v_lshrrev_b32 v6, 2, v5\n\t"
    "v_and_b32 v7, 3, v5\n\t"
    "v_lshlrev_b32 v7, 3, v7\n\t"
    "v_lshl_or_b32 v7, v6, 8, v7\n\t"
    "v_lshl_or_b32 v7, v4, 11, v7\n\t"
    "buffer_load_dwordx4 v11, s[48:51], s72 offen offset:3072 lds\n\t"
    "v_and_b32 v24, 1, v4\n\t"
    "v_lshl_or_b32 v6, v24, 2, v6\n\t"
    "s_lshl_b32 s4, s81, 1\n\t"
    "v_xor_b32 v6, s4, v6\n\t"
    "v_lshl_or_b32 v24, v6, 5, v7\n\t"
    "v_xor_b32 v25, 32, v24\n\t"
    "s_add_u32 m0, s27, 16384\n\t"
    "s_nop 0\n\t"
    "buffer_load_dwordx4 v8, s[52:55], s72 offen offset:0 lds\n\t"
    "v_xor_b32 v26, 0x80, v24\n\t"
    "v_xor_b32 v27, 0xa0, v24\n\t"
    "s_mul_i32 s4, s80, 1015808\n\t"
    "s_sub_u32 s4, 0x80000, s4\n\t"
    "v_add_u32 v12, s4, v8\n\t"
    "v_add_u32 v13, s4, v9\n\t"
    "buffer_load_dwordx4 v9, s[52:55], s72 offen offset:1024 lds\n\t"
    "v_add_u32 v14, s4, v10\n\t"
    "v_add_u32 v15, s4, v11\n\t"
    "s_sub_u32 s5, 0x80, s80\n\t"
    "v_add_u32 v6, 1, v5\n\t"
    "v_mov_b32 v7, s5\n\t"
    "v_cmp_eq_u32 vcc, 15, v5\n\t"
    "buffer_load_dwordx4 v10, s[52:55], s72 offen offset:2048 lds\n\t"
    "s_add_u32 s44, s40, 0x400\n\t"
    "s_addc_u32 s45, s41, 0\n\t"
    "v_cndmask_b32 v6, v6, v7, vcc\n\t"
    "s_sub_u32 s46, s42, 0x800\n\t"
    "s_mov_b32 s47, 0x20000\n\t"
    "v_and_b32 v7, 6, v6\n\t"
    "buffer_load_dwordx4 v11, s[52:55], s72 offen offset:3072 lds\n\t"
    "v_xor_b32 v7, v7, v4\n\t"
    "v_lshl_add_u32 v6, v6, 7, s3\n\t"
    "v_lshl_or_b32 v20, v7, 4, v6\n\t"
    "v_xor_b32 v17, 64, v16\n\t"
    "v_xor_b32 v21, 64, v20\n\t"
    "s_add_u32 m0, s27, 32768\n\t"
    "s_nop 0\n\t"
    "buffer_load_dwordx4 v8, s[48:51], s73 offen offset:0 lds\n\t"
    "buffer_load_dwordx4 v9, s[48:51], s73 offen offset:1024 lds\n\t"
    "buffer_load_dwordx4 v10, s[48:51], s73 offen offset:2048 lds\n\t"
    "buffer_load_dwordx4 v11, s[48:51], s73 offen offset:3072 lds\n\t"
    "s_sub_u32 s40, s40, 0x1000\n\t"
    "s_subb_u32 s41, s41, 0\n\t"
    "s_add_u32 s42, s42, 0x1000\n\t"
    "s_sub_u32 s44, s44, 0x1000\n\t"
    "s_subb_u32 s45, s45, 0\n\t"
    "s_add_u32 s46, s46, 0x1000\n\t"
    "s_xor_b32 s5, s82, 0\n\t"
    "s_lshl_b32 s5, s5, 11\n\t"
    "s_add_u32 s5, s5, 4096\n\t"
    "v_add_u32 v64, s5, v0\n\t"
    "v_add_u32 v65, s5, v1\n\t"
    "s_add_u32 s36, s26, s5\n\t"
    "s_sub_u32 s36, s36, 0x1000\n\t"
    "s_xor_b32 s5, s82, 1\n\t"
    "s_lshl_b32 s5, s5, 11\n\t"
    "s_add_u32 s5, s5, 2048\n\t"
    "v_add_u32 v66, s5, v0\n\t"
    "v_add_u32 v67, s5, v1\n\t"
    "s_add_u32 s37, s26, s5\n\t"
    "s_sub_u32 s37, s37, 0x1000\n\t"
    "s_xor_b32 s5, s82, 2\n\t"
    "s_lshl_b32 s5, s5, 11\n\t"
    "s_add_u32 s5, s5, 4096\n\t"
    "v_add_u32 v68, s5, v0\n\t"
    "v_add_u32 v69, s5, v1\n\t"
    "s_add_u32 s38, s26, s5\n\t"
    "s_sub_u32 s38, s38, 0x1000\n\t"
    "s_xor_b32 s5, s82, 3\n\t"
    "s_lshl_b32 s5, s5, 11\n\t"
    "s_add_u32 s5, s5, 2048\n\t"
    "v_add_u32 v70, s5, v0\n\t"
    "v_add_u32 v71, s5, v1\n\t"
    "s_add_u32 s39, s26, s5\n\t"
    "s_sub_u32 s39, s39, 0x1000\n\t"
    "s_waitcnt vmcnt(8)\n\t"
    "s_barrier\n\t"
    "ds_read_b64_tr_b16 a[72:73], v24 offset:0\n\t"
    "ds_read_b128 a[0:3], v16 offset:0\n\t"
    "ds_read_b64_tr_b16 a[74:75], v24 offset:1024\n\t"
    "ds_read_b128 a[4:7], v16 offset:2048\n\t"
    "ds_read_b64_tr_b16 a[76:77], v25 offset:0\n\t"
    "ds_read_b128 a[8:11], v16 offset:4096\n\t"
    "ds_read_b64_tr_b16 a[78:79], v25 offset:1024\n\t"
    "ds_read_b128 a[12:15], v16 offset:6144\n\t"
    "ds_read_b64_tr_b16 a[80:81], v26 offset:0\n\t"
    "ds_read_b128 a[16:19], v16 offset:8192\n\t"
    "ds_read_b64_tr_b16 a[82:83], v26 offset:1024\n\t"
    "ds_read_b128 a[20:23], v16 offset:10240\n\t"
    "ds_read_b64_tr_b16 a[84:85], v27 offset:0\n\t"
    "ds_read_b128 a[24:27], v16 offset:12288\n\t"
    "ds_read_b64_tr_b16 a[86:87], v27 offset:1024\n\t"
    "ds_read_b128 a[28:31], v16 offset:14336\n\t"
    "s_waitcnt lgkmcnt(0)\n\t"
    "v_mfma_f32_16x16x32_bf16 v[128:131], a[72:75], a[0:3], 0\n\t"
    "ds_read_b64_tr_b16 a[88:89], v24 offset:8192\n\t"
    "v_mfma_f32_16x16x32_bf16 v[144:147], a[72:75], a[4:7], 0\n\t"
    "s_add_u32 s66, s65, 0x80\n\t"
    "v_mfma_f32_16x16x32_bf16 v[148:151], a[76:79], a[4:7], 0\n\t"
    "s_add_u32 m0, s36, 32768\n\t"
    "ds_read_b128 a[32:35], v20 offset:0\n\t"
    "v_mfma_f32_16x16x32_bf16 v[132:135], a[76:79], a[0:3], 0\n\t"
    "buffer_load_dwordx4 v64, s[40:43], s65 offen offset:0 lds\n\t"
    "v_mfma_f32_16x16x32_bf16 v[136:139], a[80:83], a[0:3], 0\n\t"
    "ds_read_b64_tr_b16 a[90:91], v24 offset:9216\n\t"
    "v_mfma_f32_16x16x32_bf16 v[152:155], a[80:83], a[4:7], 0\n\t"
    "s_and_b32 s66, s66, 0x3ff\n\t"
    "v_mfma_f32_16x16x32_bf16 v[156:159], a[84:87], a[4:7], 0\n\t"
    "ds_read_b64_tr_b16 a[92:93], v25 offset:8192\n\t"
    "v_mfma_f32_16x16x32_bf16 v[140:143], a[84:87], a[0:3], 0\n\t"
    "s_lshl_b32 s74, s66, 9\n\t"
    "v_mfma_f32_16x16x32_bf16 v[168:171], a[80:83], a[8:11], 0\n\t"
    "buffer_load_dwordx4 v65, s[40:43], s65 offen offset:1024 lds\n\t"
    "v_mfma_f32_16x16x32_bf16 v[184:187], a[80:83], a[12:15], 0\n\t"
    "ds_read_b64_tr_b16 a[94:95], v25 offset:9216\n\t"
    "v_mfma_f32_16x16x32_bf16 v[188:191], a[84:87], a[12:15], 0\n\t"
    "s_add_u32 s67, s66, 0x80\n\t"
    "v_mfma_f32_16x16x32_bf16 v[172:175], a[84:87], a[8:11], 0\n\t"
    "ds_read_b64_tr_b16 a[96:97], v26 offset:8192\n\t"
    "v_mfma_f32_16x16x32_bf16 v[160:163], a[72:75], a[8:11], 0\n\t"
    "s_add_u32 m0, s37, 32768\n\t"
    "v_mfma_f32_16x16x32_bf16 v[176:179], a[72:75], a[12:15], 0\n\t"
    "buffer_load_dwordx4 v66, s[40:43], s65 offen offset:2048 lds\n\t"
    "v_mfma_f32_16x16x32_bf16 v[180:183], a[76:79], a[12:15], 0\n\t"
    "ds_read_b64_tr_b16 a[98:99], v26 offset:9216\n\t"
    "v_mfma_f32_16x16x32_bf16 v[164:167], a[76:79], a[8:11], 0\n\t"
    "s_and_b32 s67, s67, 0x3ff\n\t"
    "v_mfma_f32_16x16x32_bf16 v[192:195], a[72:75], a[16:19], 0\n\t"
    "ds_read_b64_tr_b16 a[100:101], v27 offset:8192\n\t"
    "v_mfma_f32_16x16x32_bf16 v[208:211], a[72:75], a[20:23], 0\n\t"
    "s_lshl_b32 s75, s67, 9\n\t"
    "v_mfma_f32_16x16x32_bf16 v[212:215], a[76:79], a[20:23], 0\n\t"
    "ds_read_b64_tr_b16 a[102:103], v27 offset:9216\n\t"
    "v_mfma_f32_16x16x32_bf16 v[196:199], a[76:79], a[16:19], 0\n\t"
    "buffer_load_dwordx4 v67, s[40:43], s65 offen offset:3072 lds\n\t"
    "v_mfma_f32_16x16x32_bf16 v[200:203], a[80:83], a[16:19], 0\n\t"
    "v_add_u32 v18, 0x10000, v16\n\t"
    "v_mfma_f32_16x16x32_bf16 v[216:219], a[80:83], a[20:23], 0\n\t"
    "v_add_u32 v19, 0x10000, v17\n\t"
    "v_mfma_f32_16x16x32_bf16 v[220:223], a[84:87], a[20:23], 0\n\t"
    "v_add_u32 v22, 0x10000, v20\n\t"
    "v_mfma_f32_16x16x32_bf16 v[204:207], a[84:87], a[16:19], 0\n\t"
    "s_add_u32 m0, s38, 32768\n\t"
    "v_mfma_f32_16x16x32_bf16 v[232:235], a[80:83], a[24:27], 0\n\t"
    "buffer_load_dwordx4 v68, s[40:43], s65 offen offset:0 lds\n\t"
    "v_mfma_f32_16x16x32_bf16 v[248:251], a[80:83], a[28:31], 0\n\t"
    "v_add_u32 v23, 0x10000, v21\n\t"
    "v_mfma_f32_16x16x32_bf16 v[252:255], a[84:87], a[28:31], 0\n\t"
    "v_mov_b32 v52, -1\n\t"
    "v_mfma_f32_16x16x32_bf16 v[236:239], a[84:87], a[24:27], 0\n\t"
    "v_mov_b32 v53, -1\n\t"
    "v_mfma_f32_16x16x32_bf16 v[224:227], a[72:75], a[24:27], 0\n\t"
    "v_mov_b32 v54, -1\n\t"
    "v_mfma_f32_16x16x32_bf16 v[240:243], a[72:75], a[28:31], 0\n\t"
    "buffer_load_dwordx4 v69, s[40:43], s65 offen offset:1024 lds\n\t"
    "v_mfma_f32_16x16x32_bf16 v[244:247], a[76:79], a[28:31], 0\n\t"
    "v_mov_b32 v55, -1\n\t"
    "v_mfma_f32_16x16x32_bf16 v[228:231], a[76:79], a[24:27], 0\n\t"
    "s_mov_b64 s[60:61], s[18:19]\n\t"
    "s_waitcnt vmcnt(10) lgkmcnt(0)\n\t"
    "s_barrier\n\t"
    "v_mfma_f32_16x16x32_bf16 v[128:131], a[88:91], a[4:7], v[128:131]\n\t"
    "ds_read_b64_tr_b16 a[72:73], v24 offset:16384\n\t"
    "v_mfma_f32_16x16x32_bf16 v[144:147], a[88:91], a[8:11], v[144:147]\n\t"
    "ds_read_b128 a[36:39], v17 offset:0\n\t"
    "v_mfma_f32_16x16x32_bf16 v[148:151], a[92:95], a[8:11], v[148:151]\n\t"
    "s_add_u32 m0, s39, 32768\n\t"
    "ds_read_b64_tr_b16 a[74:75], v24 offset:17408\n\t"
    "v_mfma_f32_16x16x32_bf16 v[132:135], a[92:95], a[4:7], v[132:135]\n\t"
    "buffer_load_dwordx4 v70, s[40:43], s65 offen offset:2048 lds\n\t"
    "v_mfma_f32_16x16x32_bf16 v[136:139], a[96:99], a[4:7], v[136:139]\n\t"
    "ds_read_b128 a[40:43], v17 offset:2048\n\t"
    "v_mfma_f32_16x16x32_bf16 v[152:155], a[96:99], a[8:11], v[152:155]\n\t"
    "ds_read_b64_tr_b16 a[76:77], v25 offset:16384\n\t"
    "v_mfma_f32_16x16x32_bf16 v[156:159], a[100:103], a[8:11], v[156:159]\n\t"
    "ds_read_b128 a[44:47], v17 offset:4096\n\t"
    "v_mfma_f32_16x16x32_bf16 v[140:143], a[100:103], a[4:7], v[140:143]\n\t"
    "ds_read_b64_tr_b16 a[78:79], v25 offset:17408\n\t"
    "v_mfma_f32_16x16x32_bf16 v[168:171], a[96:99], a[12:15], v[168:171]\n\t"
    "ds_read_b128 a[48:51], v17 offset:6144\n\t"
    "v_mfma_f32_16x16x32_bf16 v[184:187], a[96:99], a[16:19], v[184:187]\n\t"
    "ds_read_b64_tr_b16 a[80:81], v26 offset:16384\n\t"
    "v_mfma_f32_16x16x32_bf16 v[188:191], a[100:103], a[16:19], v[188:191]\n\t"
    "buffer_load_dwordx4 v71, s[40:43], s65 offen offset:3072 lds\n\t"
    "v_mfma_f32_16x16x32_bf16 v[172:175], a[100:103], a[12:15], v[172:175]\n\t"
    "ds_read_b128 a[52:55], v17 offset:8192\n\t"
    "v_mfma_f32_16x16x32_bf16 v[160:163], a[88:91], a[12:15], v[160:163]\n\t"
    "ds_read_b64_tr_b16 a[82:83], v26 offset:17408\n\t"
    "v_mfma_f32_16x16x32_bf16 v[176:179], a[88:91], a[16:19], v[176:179]\n\t"
    "ds_read_b128 a[56:59], v17 offset:10240\n\t"
    "v_mfma_f32_16x16x32_bf16 v[180:183], a[92:95], a[16:19], v[180:183]\n\t"
    "ds_read_b64_tr_b16 a[84:85], v27 offset:16384\n\t"
    "v_mfma_f32_16x16x32_bf16 v[164:167], a[92:95], a[12:15], v[164:167]\n\t"
    "s_add_u32 m0, s27, 49152\n\t"
    "ds_read_b128 a[60:63], v17 offset:12288\n\t"
    "v_mfma_f32_16x16x32_bf16 v[192:195], a[88:91], a[20:23], v[192:195]\n\t"
    "buffer_load_dwordx4 v8, s[52:55], s73 offen offset:0 lds\n\t"
    "v_mfma_f32_16x16x32_bf16 v[208:211], a[88:91], a[24:27], v[208:211]\n\t"
    "ds_read_b64_tr_b16 a[86:87], v27 offset:17408\n\t"
    "v_mfma_f32_16x16x32_bf16 v[212:215], a[92:95], a[24:27], v[212:215]\n\t"
    "ds_read_b128 a[64:67], v17 offset:14336\n\t"
    "v_mfma_f32_16x16x32_bf16 v[196:199], a[92:95], a[20:23], v[196:199]\n\t"
    "s_mov_b32 s62, 270336\n\t"
    "v_mfma_f32_16x16x32_bf16 v[200:203], a[96:99], a[20:23], v[200:203]\n\t"
    "s_mov_b32 s63, 0x20000\n\t"
    "v_mfma_f32_16x16x32_bf16 v[216:219], a[96:99], a[24:27], v[216:219]\n\t"
    "s_lshr_b32 s59, s59, 5\n\t"
    "v_mfma_f32_16x16x32_bf16 v[220:223], a[100:103], a[24:27], v[220:223]\n\t"
    "s_lshl2_add_u32 s58, s59, s82\n\t"
    "v_mfma_f32_16x16x32_bf16 v[204:207], a[100:103], a[20:23], v[204:207]\n\t"
    "buffer_load_dwordx4 v9, s[52:55], s73 offen offset:1024 lds\n\t"
    "v_mfma_f32_16x16x32_bf16 v[232:235], a[96:99], a[28:31], v[232:235]\n\t"
    "s_lshl_b32 s4, s58, 8\n\t"
    "v_mfma_f32_16x16x32_bf16 v[248:251], a[96:99], a[32:35], v[248:251]\n\t"
    "v_mov_b32 v28, s4\n\t"
    "v_mfma_f32_16x16x32_bf16 v[252:255], a[100:103], a[32:35], v[252:255]\n\t"
    "v_and_b32 v6, 3, v61\n\t"
    "v_mfma_f32_16x16x32_bf16 v[236:239], a[100:103], a[28:31], v[236:239]\n\t"
    "v_lshl_or_b32 v6, v4, 2, v6\n\t"
    "v_mfma_f32_16x16x32_bf16 v[224:227], a[88:91], a[28:31], v[224:227]\n\t"
    "v_bfe_u32 v7, v61, 2, 2\n\t"
    "v_mfma_f32_16x16x32_bf16 v[240:243], a[88:91], a[32:35], v[240:243]\n\t"
    "buffer_load_dwordx4 v10, s[52:55], s73 offen offset:2048 lds\n\t"
    "v_mfma_f32_16x16x32_bf16 v[244:247], a[92:95], a[32:35], v[244:247]\n\t"
    "v_bfrev_b32 v32, 1\n\t"
    "v_mfma_f32_16x16x32_bf16 v[228:231], a[92:95], a[28:31], v[228:231]\n\t"
    "s_lshl_b32 s5, s58, 5\n\t"
    "s_waitcnt lgkmcnt(0)\n\t"
    "v_mfma_f32_16x16x32_bf16 v[128:131], a[72:75], a[36:39], v[128:131]\n\t"
    "ds_read_b64_tr_b16 a[88:89], v24 offset:24576\n\t"
    "v_mfma_f32_16x16x32_bf16 v[144:147], a[72:75], a[40:43], v[144:147]\n\t"
    "s_add_u32 s5, s5, s57\n\t"
    "v_mfma_f32_16x16x32_bf16 v[148:151], a[76:79], a[40:43], v[148:151]\n\t"
    "ds_read_b128 a[68:71], v21 offset:0\n\t"
    "v_mfma_f32_16x16x32_bf16 v[132:135], a[76:79], a[36:39], v[132:135]\n\t"
    "buffer_load_dwordx4 v11, s[52:55], s73 offen offset:3072 lds\n\t"
    "v_mfma_f32_16x16x32_bf16 v[136:139], a[80:83], a[36:39], v[136:139]\n\t"
    "ds_read_b64_tr_b16 a[90:91], v24 offset:25600\n\t"
    "v_mfma_f32_16x16x32_bf16 v[152:155], a[80:83], a[40:43], v[152:155]\n\t"
    "s_lshl2_add_u32 s5, s5, s33\n\t"
    "v_mfma_f32_16x16x32_bf16 v[156:159], a[84:87], a[40:43], v[156:159]\n\t"
    "ds_read_b64_tr_b16 a[92:93], v25 offset:24576\n\t"
    "v_mfma_f32_16x16x32_bf16 v[140:143], a[84:87], a[36:39], v[140:143]\n\t"
    "s_lshl_b32 s5, s5, 8\n\t"
    "v_mfma_f32_16x16x32_bf16 v[168:171], a[80:83], a[44:47], v[168:171]\n\t"
    "ds_read_b64_tr_b16 a[94:95], v25 offset:25600\n\t"
    "v_mfma_f32_16x16x32_bf16 v[184:187], a[80:83], a[48:51], v[184:187]\n\t"
    "s_add_u32 m0, s36, 65536\n\t"
    "v_mfma_f32_16x16x32_bf16 v[188:191], a[84:87], a[48:51], v[188:191]\n\t"
    "buffer_load_dwordx4 v64, s[44:47], s64 offen offset:0 lds\n\t"
    "v_mfma_f32_16x16x32_bf16 v[172:175], a[84:87], a[44:47], v[172:175]\n\t"
    "ds_read_b64_tr_b16 a[96:97], v26 offset:24576\n\t"
    "v_mfma_f32_16x16x32_bf16 v[160:163], a[72:75], a[44:47], v[160:163]\n\t"
    "s_add_u32 s5, s5, 0x2000\n\t"
    "v_mfma_f32_16x16x32_bf16 v[176:179], a[72:75], a[48:51], v[176:179]\n\t"
    "ds_read_b64_tr_b16 a[98:99], v26 offset:25600\n\t"
    "v_mfma_f32_16x16x32_bf16 v[180:183], a[76:79], a[48:51], v[180:183]\n\t"
    "v_lshl_add_u32 v29, v6, 4, s5\n\t"
    "v_mfma_f32_16x16x32_bf16 v[164:167], a[76:79], a[44:47], v[164:167]\n\t"
    "ds_read_b64_tr_b16 a[100:101], v27 offset:24576\n\t"
    "v_mfma_f32_16x16x32_bf16 v[192:195], a[72:75], a[52:55], v[192:195]\n\t"
    "buffer_load_dwordx4 v65, s[44:47], s64 offen offset:1024 lds\n\t"
    "v_mfma_f32_16x16x32_bf16 v[208:211], a[72:75], a[56:59], v[208:211]\n\t"
    "ds_read_b64_tr_b16 a[102:103], v27 offset:25600\n\t"
    "v_mfma_f32_16x16x32_bf16 v[212:215], a[76:79], a[56:59], v[212:215]\n\t"
    "v_cmp_eq_u32 vcc, 0, v7\n\t"
    "v_mfma_f32_16x16x32_bf16 v[196:199], a[76:79], a[52:55], v[196:199]\n\t"
    "s_lshl3_add_u32 s5, s57, s4\n\t"
    "v_mfma_f32_16x16x32_bf16 v[200:203], a[80:83], a[52:55], v[200:203]\n\t"
    "s_add_u32 s5, s5, 0x1000\n\t"
    "v_mfma_f32_16x16x32_bf16 v[216:219], a[80:83], a[56:59], v[216:219]\n\t"
    "v_cndmask_b32 v29, v32, v29, vcc\n\t"
    "v_mfma_f32_16x16x32_bf16 v[220:223], a[84:87], a[56:59], v[220:223]\n\t"
    "s_add_u32 m0, s37, 65536\n\t"
    "v_mfma_f32_16x16x32_bf16 v[204:207], a[84:87], a[52:55], v[204:207]\n\t"
    "buffer_load_dwordx4 v66, s[44:47], s64 offen offset:2048 lds\n\t"
    "v_mfma_f32_16x16x32_bf16 v[232:235], a[80:83], a[60:63], v[232:235]\n\t"
    "v_mov_b32 v30, s5\n\t"
    "v_mfma_f32_16x16x32_bf16 v[248:251], a[80:83], a[64:67], v[248:251]\n\t"
    "v_cmp_eq_u32 vcc, 0, v61\n\t"
    "v_mfma_f32_16x16x32_bf16 v[252:255], a[84:87], a[64:67], v[252:255]\n\t"
    "s_add_u32 s5, s4, 0x1000\n\t"
    "v_mfma_f32_16x16x32_bf16 v[236:239], a[84:87], a[60:63], v[236:239]\n\t"
    "v_and_b32 v31, 31, v61\n\t"
    "v_mfma_f32_16x16x32_bf16 v[224:227], a[72:75], a[60:63], v[224:227]\n\t"
    "v_cndmask_b32 v30, v32, v30, vcc\n\t"
    "v_mfma_f32_16x16x32_bf16 v[240:243], a[72:75], a[64:67], v[240:243]\n\t"
    "buffer_load_dwordx4 v67, s[44:47], s64 offen offset:3072 lds\n\t"
    "v_mfma_f32_16x16x32_bf16 v[244:247], a[76:79], a[64:67], v[244:247]\n\t"
    "v_lshl_add_u32 v31, v31, 3, s5\n\t"
    "v_mfma_f32_16x16x32_bf16 v[228:231], a[76:79], a[60:63], v[228:231]\n\t"
    "s_add_u32 s68, s67, 0x80\n\t"
    "s_waitcnt vmcnt(8) lgkmcnt(0)\n\t"
    "s_barrier\n\t"
    "v_mfma_f32_16x16x32_bf16 v[128:131], a[88:91], a[40:43], v[128:131]\n\t"
    "ds_read_b64_tr_b16 a[72:73], v24 offset:32768\n\t"
    "v_mfma_f32_16x16x32_bf16 v[144:147], a[88:91], a[44:47], v[144:147]\n\t"
    "ds_read_b128 a[0:3], v16 offset:32768\n\t"
    "v_mfma_f32_16x16x32_bf16 v[148:151], a[92:95], a[44:47], v[148:151]\n\t"
    "s_add_u32 m0, s38, 65536\n\t"
    "ds_read_b64_tr_b16 a[74:75], v24 offset:33792\n\t"
    "v_mfma_f32_16x16x32_bf16 v[132:135], a[92:95], a[40:43], v[132:135]\n\t"
    "buffer_load_dwordx4 v68, s[44:47], s64 offen offset:0 lds\n\t"
    "v_mfma_f32_16x16x32_bf16 v[136:139], a[96:99], a[40:43], v[136:139]\n\t"
    "ds_read_b128 a[4:7], v16 offset:34816\n\t"
    "v_mfma_f32_16x16x32_bf16 v[152:155], a[96:99], a[44:47], v[152:155]\n\t"
    "ds_read_b64_tr_b16 a[76:77], v25 offset:32768\n\t"
    "v_mfma_f32_16x16x32_bf16 v[156:159], a[100:103], a[44:47], v[156:159]\n\t"
    "ds_read_b128 a[8:11], v16 offset:36864\n\t"
    "v_mfma_f32_16x16x32_bf16 v[140:143], a[100:103], a[40:43], v[140:143]\n\t"
    "ds_read_b64_tr_b16 a[78:79], v25 offset:33792\n\t"
    "v_mfma_f32_16x16x32_bf16 v[168:171], a[96:99], a[48:51], v[168:171]\n\t"
    "ds_read_b128 a[12:15], v16 offset:38912\n\t"
    "v_mfma_f32_16x16x32_bf16 v[184:187], a[96:99], a[52:55], v[184:187]\n\t"
    "ds_read_b64_tr_b16 a[80:81], v26 offset:32768\n\t"
    "v_mfma_f32_16x16x32_bf16 v[188:191], a[100:103], a[52:55], v[188:191]\n\t"
    "buffer_load_dwordx4 v69, s[44:47], s64 offen offset:1024 lds\n\t"
    "v_mfma_f32_16x16x32_bf16 v[172:175], a[100:103], a[48:51], v[172:175]\n\t"
    "ds_read_b128 a[16:19], v16 offset:40960\n\t"
    "v_mfma_f32_16x16x32_bf16 v[160:163], a[88:91], a[48:51], v[160:163]\n\t"
    "ds_read_b64_tr_b16 a[82:83], v26 offset:33792\n\t"
    "v_mfma_f32_16x16x32_bf16 v[176:179], a[88:91], a[52:55], v[176:179]\n\t"
    "ds_read_b128 a[20:23], v16 offset:43008\n\t"
    "v_mfma_f32_16x16x32_bf16 v[180:183], a[92:95], a[52:55], v[180:183]\n\t"
    "ds_read_b64_tr_b16 a[84:85], v27 offset:32768\n\t"
    "v_mfma_f32_16x16x32_bf16 v[164:167], a[92:95], a[48:51], v[164:167]\n\t"
    "s_add_u32 m0, s39, 65536\n\t"
    "ds_read_b128 a[24:27], v16 offset:45056\n\t"
    "v_mfma_f32_16x16x32_bf16 v[192:195], a[88:91], a[56:59], v[192:195]\n\t"
    "buffer_load_dwordx4 v70, s[44:47], s64 offen offset:2048 lds\n\t"
    "v_mfma_f32_16x16x32_bf16 v[208:211], a[88:91], a[60:63], v[208:211]\n\t"
    "ds_read_b64_tr_b16 a[86:87], v27 offset:33792\n\t"
    "v_mfma_f32_16x16x32_bf16 v[212:215], a[92:95], a[60:63], v[212:215]\n\t"
    "ds_read_b128 a[28:31], v16 offset:47104\n\t"
    "v_mfma_f32_16x16x32_bf16 v[196:199], a[92:95], a[56:59], v[196:199]\n\t"
    "s_and_b32 s68, s68, 0x3ff\n\t"
    "v_mfma_f32_16x16x32_bf16 v[200:203], a[96:99], a[56:59], v[200:203]\n\t"
    "s_lshl_b32 s76, s68, 9\n\t"
    "v_mfma_f32_16x16x32_bf16 v[216:219], a[96:99], a[60:63], v[216:219]\n\t"
    "s_add_u32 s69, s68, 0x80\n\t"
    "v_mfma_f32_16x16x32_bf16 v[220:223], a[100:103], a[60:63], v[220:223]\n\t"
    "s_and_b32 s69, s69, 0x3ff\n\t"
    "v_mfma_f32_16x16x32_bf16 v[204:207], a[100:103], a[56:59], v[204:207]\n\t"
    "buffer_load_dwordx4 v71, s[44:47], s64 offen offset:3072 lds\n\t"
    "v_mfma_f32_16x16x32_bf16 v[232:235], a[96:99], a[64:67], v[232:235]\n\t"
    "s_lshl_b32 s77, s69, 9\n\t"
    "v_mfma_f32_16x16x32_bf16 v[248:251], a[96:99], a[68:71], v[248:251]\n\t"
    "s_cmp_eq_u32 s80, 0\n\t"
    "s_cselect_b64 s[6:7], -1, 0\n\t"
    "v_mfma_f32_16x16x32_bf16 v[252:255], a[100:103], a[68:71], v[252:255]\n\t"
    "v_cndmask_b32 v33, v32, v29, s[6:7]\n\t"
    "v_mfma_f32_16x16x32_bf16 v[236:239], a[100:103], a[64:67], v[236:239]\n\t"
    "v_cndmask_b32 v34, v29, v32, s[6:7]\n\t"
    "v_mfma_f32_16x16x32_bf16 v[224:227], a[88:91], a[64:67], v[224:227]\n\t"
    "s_add_u32 m0, s27, 0\n\t"
    "v_mfma_f32_16x16x32_bf16 v[240:243], a[88:91], a[68:71], v[240:243]\n\t"
    "buffer_load_dwordx4 v12, s[48:51], s72 offen offset:0 lds\n\t"
    "v_mfma_f32_16x16x32_bf16 v[244:247], a[92:95], a[68:71], v[244:247]\n\t"
    "v_and_b32 v62, 63, v61\n\t"
    "v_mfma_f32_16x16x32_bf16 v[228:231], a[92:95], a[64:67], v[228:231]\n\t"
    "s_lshl_b32 s5, s33, 16\n\t"
    "s_waitcnt lgkmcnt(0)\n\t"
    "v_mfma_f32_16x16x32_bf16 v[128:131], a[72:75], a[0:3], v[128:131]\n\t"
    "ds_read_b64_tr_b16 a[88:89], v24 offset:40960\n\t"
    "v_mfma_f32_16x16x32_bf16 v[144:147], a[72:75], a[4:7], v[144:147]\n\t"
    "v_cmp_gt_u32 s[6:7], 1, v62\n\t"
    "v_mfma_f32_16x16x32_bf16 v[148:151], a[76:79], a[4:7], v[148:151]\n\t"
    "ds_read_b128 a[32:35], v20 offset:32768\n\t"
    "v_mfma_f32_16x16x32_bf16 v[132:135], a[76:79], a[0:3], v[132:135]\n\t"
    "buffer_load_dwordx4 v13, s[48:51], s72 offen offset:1024 lds\n\t"
    "v_mfma_f32_16x16x32_bf16 v[136:139], a[80:83], a[0:3], v[136:139]\n\t"
    "ds_read_b64_tr_b16 a[90:91], v24 offset:41984\n\t"
    "v_mfma_f32_16x16x32_bf16 v[152:155], a[80:83], a[4:7], v[152:155]\n\t"
    "v_lshl_add_u32 v62, v62, 16, s5\n\t"
    "v_mfma_f32_16x16x32_bf16 v[156:159], a[84:87], a[4:7], v[156:159]\n\t"
    "ds_read_b64_tr_b16 a[92:93], v25 offset:40960\n\t"
    "v_mfma_f32_16x16x32_bf16 v[140:143], a[84:87], a[0:3], v[140:143]\n\t"
    "s_cmp_eq_u32 s57, 0\n\t"
    "s_cselect_b32 s5, s4, 0x80000000\n\t"
    "v_mfma_f32_16x16x32_bf16 v[168:171], a[80:83], a[8:11], v[168:171]\n\t"
    "ds_read_b64_tr_b16 a[94:95], v25 offset:41984\n\t"
    "v_mfma_f32_16x16x32_bf16 v[184:187], a[80:83], a[12:15], v[184:187]\n\t"
    "v_cndmask_b32 v62, v32, v62, s[6:7]\n\t"
    "v_mfma_f32_16x16x32_bf16 v[188:191], a[84:87], a[12:15], v[188:191]\n\t"
    "buffer_load_dwordx4 v14, s[48:51], s72 offen offset:2048 lds\n\t"
    "v_mfma_f32_16x16x32_bf16 v[172:175], a[84:87], a[8:11], v[172:175]\n\t"
    "ds_read_b64_tr_b16 a[96:97], v26 offset:40960\n\t"
    "v_mfma_f32_16x16x32_bf16 v[160:163], a[72:75], a[8:11], v[160:163]\n\t"
    "v_mov_b32 v35, s5\n\t"
    "v_mfma_f32_16x16x32_bf16 v[176:179], a[72:75], a[12:15], v[176:179]\n\t"
    "ds_read_b64_tr_b16 a[98:99], v26 offset:41984\n\t"
    "v_mfma_f32_16x16x32_bf16 v[180:183], a[76:79], a[12:15], v[180:183]\n\t"
    "v_cndmask_b32 v32, v32, v35, vcc\n\t"
    "v_mfma_f32_16x16x32_bf16 v[164:167], a[76:79], a[8:11], v[164:167]\n\t"
    "ds_read_b64_tr_b16 a[100:101], v27 offset:40960\n\t"
    "v_mfma_f32_16x16x32_bf16 v[192:195], a[72:75], a[16:19], v[192:195]\n\t"
    "buffer_load_dwordx4 v15, s[48:51], s72 offen offset:3072 lds\n\t"
    "v_mfma_f32_16x16x32_bf16 v[208:211], a[72:75], a[20:23], v[208:211]\n\t"
    "ds_read_b64_tr_b16 a[102:103], v27 offset:41984\n\t"
    "v_mfma_f32_16x16x32_bf16 v[212:215], a[76:79], a[20:23], v[212:215]\n\t"
    "s_lshl2_add_u32 s5, s82, s81\n\t"
    "v_mfma_f32_16x16x32_bf16 v[196:199], a[76:79], a[16:19], v[196:199]\n\t"
    "s_lshl_b32 s5, s5, 6\n\t"
    "v_mfma_f32_16x16x32_bf16 v[200:203], a[80:83], a[16:19], v[200:203]\n\t"
    "v_lshl_add_u32 v35, v6, 2, s5\n\t"
    "v_mfma_f32_16x16x32_bf16 v[216:219], a[80:83], a[20:23], v[216:219]\n\t"
    "s_lshr_b32 s4, s2, 3\n\t"
    "v_mfma_f32_16x16x32_bf16 v[220:223], a[84:87], a[20:23], v[220:223]\n\t"
    "s_add_u32 m0, s36, 0\n\t"
    "v_mfma_f32_16x16x32_bf16 v[204:207], a[84:87], a[16:19], v[204:207]\n\t"
    "buffer_load_dwordx4 v64, s[44:47], s65 offen offset:0 lds\n\t"
    "v_mfma_f32_16x16x32_bf16 v[232:235], a[80:83], a[24:27], v[232:235]\n\t"
    "s_lshl2_add_u32 s4, s4, s33\n\t"
    "v_mfma_f32_16x16x32_bf16 v[248:251], a[80:83], a[28:31], v[248:251]\n\t"
    "s_and_b32 s4, s4, 63\n\t"
    "v_mfma_f32_16x16x32_bf16 v[252:255], a[84:87], a[28:31], v[252:255]\n\t"
    "s_lshl_b32 s4, s4, 7\n\t"
    "v_mfma_f32_16x16x32_bf16 v[236:239], a[84:87], a[24:27], v[236:239]\n\t"
    "v_mov_b32 v60, s4\n\t"
    "v_mfma_f32_16x16x32_bf16 v[224:227], a[72:75], a[24:27], v[224:227]\n\t"
    "s_getpc_b64 s[34:35]\n\t"
    "L_pf2_%=:\n\t"
    "s_add_u32 s34, s34, L_kernel_end_marker-L_pf2_%=\n\t"
    "s_addc_u32 s35, s35, 0\n\t"
    "v_mfma_f32_16x16x32_bf16 v[240:243], a[72:75], a[28:31], v[240:243]\n\t"
    "buffer_load_dwordx4 v65, s[44:47], s65 offen offset:1024 lds\n\t"
    "v_mfma_f32_16x16x32_bf16 v[244:247], a[76:79], a[28:31], v[244:247]\n\t"
    "s_add_u32 s70, s69, 0x80\n\t"
    "v_mfma_f32_16x16x32_bf16 v[228:231], a[76:79], a[24:27], v[228:231]\n\t"
    "s_and_b32 s70, s70, 0x3ff\n\t"
    "s_waitcnt vmcnt(14) lgkmcnt(0)\n\t"
    "s_barrier\n\t"
    "v_mfma_f32_16x16x32_bf16 v[128:131], a[88:91], a[4:7], v[128:131]\n\t"
    "ds_read_b64_tr_b16 a[72:73], v24 offset:49152\n\t"
    "v_mfma_f32_16x16x32_bf16 v[144:147], a[88:91], a[8:11], v[144:147]\n\t"
    "ds_read_b128 a[36:39], v17 offset:32768\n\t"
    "v_mfma_f32_16x16x32_bf16 v[148:151], a[92:95], a[8:11], v[148:151]\n\t"
    "s_add_u32 m0, s37, 0\n\t"
    "ds_read_b64_tr_b16 a[74:75], v24 offset:50176\n\t"
    "v_mfma_f32_16x16x32_bf16 v[132:135], a[92:95], a[4:7], v[132:135]\n\t"
    "buffer_load_dwordx4 v66, s[44:47], s65 offen offset:2048 lds\n\t"
    "v_mfma_f32_16x16x32_bf16 v[136:139], a[96:99], a[4:7], v[136:139]\n\t"
    "ds_read_b128 a[40:43], v17 offset:34816\n\t"
    "v_mfma_f32_16x16x32_bf16 v[152:155], a[96:99], a[8:11], v[152:155]\n\t"
    "ds_read_b64_tr_b16 a[76:77], v25 offset:49152\n\t"
    "v_mfma_f32_16x16x32_bf16 v[156:159], a[100:103], a[8:11], v[156:159]\n\t"
    "ds_read_b128 a[44:47], v17 offset:36864\n\t"
    "v_mfma_f32_16x16x32_bf16 v[140:143], a[100:103], a[4:7], v[140:143]\n\t"
    "ds_read_b64_tr_b16 a[78:79], v25 offset:50176\n\t"
    "v_mfma_f32_16x16x32_bf16 v[168:171], a[96:99], a[12:15], v[168:171]\n\t"
    "ds_read_b128 a[48:51], v17 offset:38912\n\t"
    "v_mfma_f32_16x16x32_bf16 v[184:187], a[96:99], a[16:19], v[184:187]\n\t"
    "ds_read_b64_tr_b16 a[80:81], v26 offset:49152\n\t"
    "v_mfma_f32_16x16x32_bf16 v[188:191], a[100:103], a[16:19], v[188:191]\n\t"
    "buffer_load_dwordx4 v67, s[44:47], s65 offen offset:3072 lds\n\t"
    "v_mfma_f32_16x16x32_bf16 v[172:175], a[100:103], a[12:15], v[172:175]\n\t"
    "ds_read_b128 a[52:55], v17 offset:40960\n\t"
    "v_mfma_f32_16x16x32_bf16 v[160:163], a[88:91], a[12:15], v[160:163]\n\t"
    "ds_read_b64_tr_b16 a[82:83], v26 offset:50176\n\t"
    "v_mfma_f32_16x16x32_bf16 v[176:179], a[88:91], a[16:19], v[176:179]\n\t"
    "ds_read_b128 a[56:59], v17 offset:43008\n\t"
    "v_mfma_f32_16x16x32_bf16 v[180:183], a[92:95], a[16:19], v[180:183]\n\t"
    "ds_read_b64_tr_b16 a[84:85], v27 offset:49152\n\t"
    "v_mfma_f32_16x16x32_bf16 v[164:167], a[92:95], a[12:15], v[164:167]\n\t"
    "s_add_u32 m0, s38, 0\n\t"
    "ds_read_b128 a[60:63], v17 offset:45056\n\t"
    "v_mfma_f32_16x16x32_bf16 v[192:195], a[88:91], a[20:23], v[192:195]\n\t"
    "buffer_load_dwordx4 v68, s[44:47], s65 offen offset:0 lds\n\t"
    "v_mfma_f32_16x16x32_bf16 v[208:211], a[88:91], a[24:27], v[208:211]\n\t"
    "ds_read_b64_tr_b16 a[86:87], v27 offset:50176\n\t"
    "v_mfma_f32_16x16x32_bf16 v[212:215], a[92:95], a[24:27], v[212:215]\n\t"
    "ds_read_b128 a[64:67], v17 offset:47104\n\t"
    "v_mfma_f32_16x16x32_bf16 v[196:199], a[92:95], a[20:23], v[196:199]\n\t"
    "s_lshl_b32 s78, s70, 9\n\t"
    "v_mfma_f32_16x16x32_bf16 v[200:203], a[96:99], a[20:23], v[200:203]\n\t"
    "s_add_u32 s71, s70, 0x80\n\t"
    "v_mfma_f32_16x16x32_bf16 v[216:219], a[96:99], a[24:27], v[216:219]\n\t"
    "s_and_b32 s71, s71, 0x3ff\n\t"
    "v_mfma_f32_16x16x32_bf16 v[220:223], a[100:103], a[24:27], v[220:223]\n\t"
    "s_lshl_b32 s79, s71, 9\n\t"
    "v_mfma_f32_16x16x32_bf16 v[204:207], a[100:103], a[20:23], v[204:207]\n\t"
    "buffer_load_dwordx4 v69, s[44:47], s65 offen offset:1024 lds\n\t"
    "v_mfma_f32_16x16x32_bf16 v[232:235], a[96:99], a[28:31], v[232:235]\n\t"
    "s_mul_i32 s4, s59, 7999\n\t"
    "v_mfma_f32_16x16x32_bf16 v[248:251], a[96:99], a[32:35], v[248:251]\n\t"
    "s_mul_i32 s5, s57, 250\n\t"
    "v_mfma_f32_16x16x32_bf16 v[252:255], a[100:103], a[32:35], v[252:255]\n\t"
    "s_add_u32 s4, s4, s5\n\t"
    "v_mfma_f32_16x16x32_bf16 v[236:239], a[100:103], a[28:31], v[236:239]\n\t"
    "s_lshl_b32 s4, s4, 10\n\t"
    "v_mfma_f32_16x16x32_bf16 v[224:227], a[88:91], a[28:31], v[224:227]\n\t"
    "s_add_u32 m0, s39, 0\n\t"
    "v_mfma_f32_16x16x32_bf16 v[240:243], a[88:91], a[32:35], v[240:243]\n\t"
    "buffer_load_dwordx4 v70, s[44:47], s65 offen offset:2048 lds\n\t"
    "v_mfma_f32_16x16x32_bf16 v[244:247], a[92:95], a[32:35], v[244:247]\n\t"
    "s_add_u32 s4, s4, s23\n\t"
    "v_mfma_f32_16x16x32_bf16 v[228:231], a[92:95], a[28:31], v[228:231]\n\t"
    "s_add_u32 s84, s16, s4\n\t"
    "s_addc_u32 s85, s17, 0\n\t"
    "s_waitcnt lgkmcnt(0)\n\t"
    "v_mfma_f32_16x16x32_bf16 v[128:131], a[72:75], a[36:39], v[128:131]\n\t"
    "ds_read_b64_tr_b16 a[88:89], v24 offset:57344\n\t"
    "v_mfma_f32_16x16x32_bf16 v[144:147], a[72:75], a[40:43], v[144:147]\n\t"
    "s_lshr_b32 s4, s21, 1\n\t"
    "v_mfma_f32_16x16x32_bf16 v[148:151], a[76:79], a[40:43], v[148:151]\n\t"
    "ds_read_b128 a[68:71], v21 offset:32768\n\t"
    "v_mfma_f32_16x16x32_bf16 v[132:135], a[76:79], a[36:39], v[132:135]\n\t"
    "buffer_load_dwordx4 v71, s[44:47], s65 offen offset:3072 lds\n\t"
    "v_mfma_f32_16x16x32_bf16 v[136:139], a[80:83], a[36:39], v[136:139]\n\t"
    "ds_read_b64_tr_b16 a[90:91], v24 offset:58368\n\t"
    "v_mfma_f32_16x16x32_bf16 v[152:155], a[80:83], a[40:43], v[152:155]\n\t"
    "s_sub_u32 s86, 255232, s4\n\t"
    "v_mfma_f32_16x16x32_bf16 v[156:159], a[84:87], a[40:43], v[156:159]\n\t"
    "ds_read_b64_tr_b16 a[92:93], v25 offset:57344\n\t"
    "v_mfma_f32_16x16x32_bf16 v[140:143], a[84:87], a[36:39], v[140:143]\n\t"
    "s_mov_b32 s87, 0x20000\n\t"
    "v_mfma_f32_16x16x32_bf16 v[168:171], a[80:83], a[44:47], v[168:171]\n\t"
    "ds_read_b64_tr_b16 a[94:95], v25 offset:58368\n\t"
    "v_mfma_f32_16x16x32_bf16 v[184:187], a[80:83], a[48:51], v[184:187]\n\t"
    "s_add_u32 m0, s27, 16384\n\t"
    "v_mfma_f32_16x16x32_bf16 v[188:191], a[84:87], a[48:51], v[188:191]\n\t"
    "buffer_load_dwordx4 v12, s[48:51], s73 offen offset:0 lds\n\t"
    "v_mfma_f32_16x16x32_bf16 v[172:175], a[84:87], a[44:47], v[172:175]\n\t"
    "ds_read_b64_tr_b16 a[96:97], v26 offset:57344\n\t"
    "v_mfma_f32_16x16x32_bf16 v[160:163], a[72:75], a[44:47], v[160:163]\n\t"
    "s_cmp_eq_u32 s80, 0\n\t"
    "s_cselect_b32 s4, -1, 0x7fff7fff\n\t"
    "v_mfma_f32_16x16x32_bf16 v[176:179], a[72:75], a[48:51], v[176:179]\n\t"
    "ds_read_b64_tr_b16 a[98:99], v26 offset:58368\n\t"
    "v_mfma_f32_16x16x32_bf16 v[180:183], a[76:79], a[48:51], v[180:183]\n\t"
    "s_cmp_eq_u32 s21, 0\n\t"
    "s_cselect_b32 s28, -1, s4\n\t"
    "s_cselect_b32 s30, s4, -1\n\t"
    "v_mfma_f32_16x16x32_bf16 v[164:167], a[76:79], a[44:47], v[164:167]\n\t"
    "ds_read_b64_tr_b16 a[100:101], v27 offset:57344\n\t"
    "v_mfma_f32_16x16x32_bf16 v[192:195], a[72:75], a[52:55], v[192:195]\n\t"
    "buffer_load_dwordx4 v13, s[48:51], s73 offen offset:1024 lds\n\t"
    "v_mfma_f32_16x16x32_bf16 v[208:211], a[72:75], a[56:59], v[208:211]\n\t"
    "ds_read_b64_tr_b16 a[102:103], v27 offset:58368\n\t"
    "v_mfma_f32_16x16x32_bf16 v[212:215], a[76:79], a[56:59], v[212:215]\n\t"
    "s_mov_b32 s29, s28\n\t"
    "v_mfma_f32_16x16x32_bf16 v[196:199], a[76:79], a[52:55], v[196:199]\n\t"
    "s_mov_b32 s31, s30\n\t"
    "v_mfma_f32_16x16x32_bf16 v[200:203], a[80:83], a[52:55], v[200:203]\n\t"
    "s_lshr_b32 s83, s57, 2\n\t"
    "v_mfma_f32_16x16x32_bf16 v[216:219], a[80:83], a[56:59], v[216:219]\n\t"
    "s_lshl_b32 s83, s83, 12\n\t"
    "v_mfma_f32_16x16x32_bf16 v[220:223], a[84:87], a[56:59], v[220:223]\n\t"
    "v_xor_b32 v7, s57, v7\n\t"
    "v_mfma_f32_16x16x32_bf16 v[204:207], a[84:87], a[52:55], v[204:207]\n\t"
    "buffer_load_dwordx4 v14, s[48:51], s73 offen offset:2048 lds\n\t"
    "v_mfma_f32_16x16x32_bf16 v[232:235], a[80:83], a[60:63], v[232:235]\n\t"
    "v_and_b32 v7, 3, v7\n\t"
    "v_mfma_f32_16x16x32_bf16 v[248:251], a[80:83], a[64:67], v[248:251]\n\t"
    "s_lshl_b32 s4, s58, 7\n\t"
    "v_mfma_f32_16x16x32_bf16 v[252:255], a[84:87], a[64:67], v[252:255]\n\t"
    "s_add_u32 s4, s4, s81\n\t"
    "v_mfma_f32_16x16x32_bf16 v[236:239], a[84:87], a[60:63], v[236:239]\n\t"
    "s_lshl_b32 s4, s4, 8\n\t"
    "v_mfma_f32_16x16x32_bf16 v[224:227], a[72:75], a[60:63], v[224:227]\n\t"
    "s_add_u32 s4, s4, 0x2000\n\t"
    "v_mfma_f32_16x16x32_bf16 v[240:243], a[72:75], a[64:67], v[240:243]\n\t"
    "buffer_load_dwordx4 v15, s[48:51], s73 offen offset:3072 lds\n\t"
    "v_mfma_f32_16x16x32_bf16 v[244:247], a[76:79], a[64:67], v[244:247]\n\t"
    "v_lshl_add_u32 v7, v7, 10, s4\n\t"
    "v_mfma_f32_16x16x32_bf16 v[228:231], a[76:79], a[60:63], v[228:231]\n\t"
    "v_lshl_add_u32 v38, v6, 4, v7\n\t"
    "s_waitcnt vmcnt(12) lgkmcnt(0)\n\t"
    "s_barrier\n\t"
    "v_mfma_f32_16x16x32_bf16 v[128:131], a[88:91], a[40:43], v[128:131]\n\t"
    "ds_read_b64_tr_b16 a[72:73], v24 offset:0\n\t"
    "v_mfma_f32_16x16x32_bf16 v[144:147], a[88:91], a[44:47], v[144:147]\n\t"
    "s_add_u32 m0, s27, 32768\n\t"
    "ds_read_b128 a[0:3], v18 offset:0\n\t"
    "v_mfma_f32_16x16x32_bf16 v[148:151], a[92:95], a[44:47], v[148:151]\n\t"
    "buffer_load_dwordx4 v8, s[48:51], s74 offen offset:0 lds\n\t"
    "v_mfma_f32_16x16x32_bf16 v[132:135], a[92:95], a[40:43], v[132:135]\n\t"
    "ds_read_b64_tr_b16 a[74:75], v24 offset:1024\n\t"
    "v_mfma_f32_16x16x32_bf16 v[136:139], a[96:99], a[40:43], v[136:139]\n\t"
    "ds_read_b128 a[4:7], v18 offset:2048\n\t"
    "v_mfma_f32_16x16x32_bf16 v[152:155], a[96:99], a[44:47], v[152:155]\n\t"
    "ds_read_b64_tr_b16 a[76:77], v25 offset:0\n\t"
    "v_mfma_f32_16x16x32_bf16 v[156:159], a[100:103], a[44:47], v[156:159]\n\t"
    "ds_read_b128 a[8:11], v18 offset:4096\n\t"
    "v_mfma_f32_16x16x32_bf16 v[140:143], a[100:103], a[40:43], v[140:143]\n\t"
    "buffer_load_dwordx4 v9, s[48:51], s74 offen offset:1024 lds\n\t"
    "v_mfma_f32_16x16x32_bf16 v[168:171], a[96:99], a[48:51], v[168:171]\n\t"
    "ds_read_b64_tr_b16 a[78:79], v25 offset:1024\n\t"
    "v_mfma_f32_16x16x32_bf16 v[184:187], a[96:99], a[52:55], v[184:187]\n\t"
    "ds_read_b128 a[12:15], v18 offset:6144\n\t"
    "v_mfma_f32_16x16x32_bf16 v[188:191], a[100:103], a[52:55], v[188:191]\n\t"
    "ds_read_b64_tr_b16 a[80:81], v26 offset:0\n\t"
    "v_mfma_f32_16x16x32_bf16 v[172:175], a[100:103], a[48:51], v[172:175]\n\t"
    "buffer_load_dwordx4 v10, s[48:51], s74 offen offset:2048 lds\n\t"
    "v_mfma_f32_16x16x32_bf16 v[160:163], a[88:91], a[48:51], v[160:163]\n\t"
    "ds_read_b128 a[16:19], v18 offset:8192\n\t"
    "v_mfma_f32_16x16x32_bf16 v[176:179], a[88:91], a[52:55], v[176:179]\n\t"
    "ds_read_b64_tr_b16 a[82:83], v26 offset:1024\n\t"
    "v_mfma_f32_16x16x32_bf16 v[180:183], a[92:95], a[52:55], v[180:183]\n\t"
    "ds_read_b128 a[20:23], v18 offset:10240\n\t"
    "v_mfma_f32_16x16x32_bf16 v[164:167], a[92:95], a[48:51], v[164:167]\n\t"
    "ds_read_b64_tr_b16 a[84:85], v27 offset:0\n\t"
    "v_mfma_f32_16x16x32_bf16 v[192:195], a[88:91], a[56:59], v[192:195]\n\t"
    "buffer_load_dwordx4 v11, s[48:51], s74 offen offset:3072 lds\n\t"
    "v_mfma_f32_16x16x32_bf16 v[208:211], a[88:91], a[60:63], v[208:211]\n\t"
    "ds_read_b128 a[24:27], v18 offset:12288\n\t"
    "v_mfma_f32_16x16x32_bf16 v[212:215], a[92:95], a[60:63], v[212:215]\n\t"
    "ds_read_b64_tr_b16 a[86:87], v27 offset:1024\n\t"
    "v_mfma_f32_16x16x32_bf16 v[196:199], a[92:95], a[56:59], v[196:199]\n\t"
    "ds_read_b128 a[28:31], v18 offset:14336\n\t"
    "v_mfma_f32_16x16x32_bf16 v[200:203], a[96:99], a[56:59], v[200:203]\n\t"
    "s_add_u32 m0, s36, 32768\n\t"
    "v_mfma_f32_16x16x32_bf16 v[216:219], a[96:99], a[60:63], v[216:219]\n\t"
    "buffer_load_dwordx4 v64, s[40:43], s66 offen offset:0 lds\n\t"
    "v_mfma_f32_16x16x32_bf16 v[220:223], a[100:103], a[60:63], v[220:223]\n\t"
    "s_lshl_b32 s4, s81, 7\n\t"
    "v_mfma_f32_16x16x32_bf16 v[204:207], a[100:103], a[56:59], v[204:207]\n\t"
    "s_add_u32 s4, s4, 98304\n\t"
    "v_mfma_f32_16x16x32_bf16 v[232:235], a[96:99], a[64:67], v[232:235]\n\t"
    "v_lshl_add_u32 v37, v6, 3, s4\n\t"
    "v_mfma_f32_16x16x32_bf16 v[248:251], a[96:99], a[68:71], v[248:251]\n\t"
    "buffer_load_dwordx4 v65, s[40:43], s66 offen offset:1024 lds\n\t"
    "v_mfma_f32_16x16x32_bf16 v[252:255], a[100:103], a[68:71], v[252:255]\n\t"
    "s_lshl_b32 s5, s33, 8\n\t"
    "v_mfma_f32_16x16x32_bf16 v[236:239], a[100:103], a[64:67], v[236:239]\n\t"
    "s_add_u32 s5, s5, 100352\n\t"
    "v_mfma_f32_16x16x32_bf16 v[224:227], a[88:91], a[64:67], v[224:227]\n\t"
    "v_lshl_add_u32 v39, v6, 4, s5\n\t"
    "v_mfma_f32_16x16x32_bf16 v[240:243], a[88:91], a[68:71], v[240:243]\n\t"
    "s_add_u32 m0, s37, 32768\n\t"
    "v_mfma_f32_16x16x32_bf16 v[244:247], a[92:95], a[68:71], v[244:247]\n\t"
    "buffer_load_dwordx4 v66, s[40:43], s66 offen offset:2048 lds\n\t"
    "v_mfma_f32_16x16x32_bf16 v[228:231], a[92:95], a[64:67], v[228:231]\n\t"
    "v_lshl_add_u32 v40, v4, 5, s5\n\t"
    "s_waitcnt lgkmcnt(0)\n\t"
    "v_mfma_f32_16x16x32_bf16 v[128:131], a[72:75], a[0:3], v[128:131]\n\t"
    "ds_read_b64_tr_b16 a[88:89], v24 offset:8192\n\t"
    "v_mfma_f32_16x16x32_bf16 v[144:147], a[72:75], a[4:7], v[144:147]\n\t"
    "ds_read_b128 a[36:39], v19 offset:0\n\t"
    "v_mfma_f32_16x16x32_bf16 v[148:151], a[76:79], a[4:7], v[148:151]\n\t"
    "buffer_load_dwordx4 v67, s[40:43], s66 offen offset:3072 lds\n\t"
    "v_mfma_f32_16x16x32_bf16 v[132:135], a[76:79], a[0:3], v[132:135]\n\t"
    "ds_read_b64_tr_b16 a[90:91], v24 offset:9216\n\t"
    "v_mfma_f32_16x16x32_bf16 v[136:139], a[80:83], a[0:3], v[136:139]\n\t"
    "ds_read_b128 a[40:43], v19 offset:2048\n\t"
    "v_mfma_f32_16x16x32_bf16 v[152:155], a[80:83], a[4:7], v[152:155]\n\t"
    "ds_read_b64_tr_b16 a[92:93], v25 offset:8192\n\t"
    "v_mfma_f32_16x16x32_bf16 v[156:159], a[84:87], a[4:7], v[156:159]\n\t"
    "s_add_u32 m0, s38, 32768\n\t"
    "ds_read_b128 a[44:47], v19 offset:4096\n\t"
    "v_mfma_f32_16x16x32_bf16 v[140:143], a[84:87], a[0:3], v[140:143]\n\t"
    "buffer_load_dwordx4 v68, s[40:43], s66 offen offset:0 lds\n\t"
    "v_mfma_f32_16x16x32_bf16 v[168:171], a[80:83], a[8:11], v[168:171]\n\t"
    "ds_read_b64_tr_b16 a[94:95], v25 offset:9216\n\t"
    "v_mfma_f32_16x16x32_bf16 v[184:187], a[80:83], a[12:15], v[184:187]\n\t"
    "ds_read_b128 a[48:51], v19 offset:6144\n\t"
    "v_mfma_f32_16x16x32_bf16 v[188:191], a[84:87], a[12:15], v[188:191]\n\t"
    "ds_read_b64_tr_b16 a[96:97], v26 offset:8192\n\t"
    "v_mfma_f32_16x16x32_bf16 v[172:175], a[84:87], a[8:11], v[172:175]\n\t"
    "buffer_load_dwordx4 v69, s[40:43], s66 offen offset:1024 lds\n\t"
    "v_mfma_f32_16x16x32_bf16 v[160:163], a[72:75], a[8:11], v[160:163]\n\t"
    "ds_read_b128 a[52:55], v19 offset:8192\n\t"
    "v_mfma_f32_16x16x32_bf16 v[176:179], a[72:75], a[12:15], v[176:179]\n\t"
    "ds_read_b64_tr_b16 a[98:99], v26 offset:9216\n\t"
    "v_mfma_f32_16x16x32_bf16 v[180:183], a[76:79], a[12:15], v[180:183]\n\t"
    "ds_read_b128 a[56:59], v19 offset:10240\n\t"
    "v_mfma_f32_16x16x32_bf16 v[164:167], a[76:79], a[8:11], v[164:167]\n\t"
    "s_add_u32 m0, s39, 32768\n\t"
    "ds_read_b64_tr_b16 a[100:101], v27 offset:8192\n\t"
    "v_mfma_f32_16x16x32_bf16 v[192:195], a[72:75], a[16:19], v[192:195]\n\t"
    "buffer_load_dwordx4 v70, s[40:43], s66 offen offset:2048 lds\n\t"
    "v_mfma_f32_16x16x32_bf16 v[208:211], a[72:75], a[20:23], v[208:211]\n\t"
    "ds_read_b128 a[60:63], v19 offset:12288\n\t"
    "v_mfma_f32_16x16x32_bf16 v[212:215], a[76:79], a[20:23], v[212:215]\n\t"
    "ds_read_b64_tr_b16 a[102:103], v27 offset:9216\n\t"
    "v_mfma_f32_16x16x32_bf16 v[196:199], a[76:79], a[16:19], v[196:199]\n\t"
    "ds_read_b128 a[64:67], v19 offset:14336\n\t"
    "v_mfma_f32_16x16x32_bf16 v[200:203], a[80:83], a[16:19], v[200:203]\n\t"
    "s_lshl_b32 s5, s80, 9\n\t"
    "v_mfma_f32_16x16x32_bf16 v[216:219], a[80:83], a[20:23], v[216:219]\n\t"
    "buffer_load_dwordx4 v71, s[40:43], s66 offen offset:3072 lds\n\t"
    "v_mfma_f32_16x16x32_bf16 v[220:223], a[84:87], a[20:23], v[220:223]\n\t"
    "s_add_u32 s4, s4, s5\n\t"
    "v_mfma_f32_16x16x32_bf16 v[204:207], a[84:87], a[16:19], v[204:207]\n\t"
    "v_and_b32 v6, 8, v5\n\t"
    "v_mfma_f32_16x16x32_bf16 v[232:235], a[80:83], a[24:27], v[232:235]\n\t"
    "v_and_b32 v7, 4, v5\n\t"
    "v_mfma_f32_16x16x32_bf16 v[248:251], a[80:83], a[28:31], v[248:251]\n\t"
    "buffer_load_dwordx2 v[48:49], v28, s[60:63], 0 offen sc1\n\t"
    "v_mfma_f32_16x16x32_bf16 v[252:255], a[84:87], a[28:31], v[252:255]\n\t"
    "v_lshl_add_u32 v6, v6, 5, s4\n\t"
    "v_mfma_f32_16x16x32_bf16 v[236:239], a[84:87], a[24:27], v[236:239]\n\t"
    "v_lshl_add_u32 v6, v7, 4, v6\n\t"
    "v_mfma_f32_16x16x32_bf16 v[224:227], a[72:75], a[24:27], v[224:227]\n\t"
    "v_lshl_add_u32 v36, v4, 4, v6\n\t"
    "v_mfma_f32_16x16x32_bf16 v[240:243], a[72:75], a[28:31], v[240:243]\n\t"
    "s_lshl_b32 s4, s80, 7\n\t"
    "v_mfma_f32_16x16x32_bf16 v[244:247], a[76:79], a[28:31], v[244:247]\n\t"
    "buffer_store_dwordx4 v[52:55], v29, s[60:63], 0 offen sc1\n\t"
    "v_mfma_f32_16x16x32_bf16 v[228:231], a[76:79], a[24:27], v[228:231]\n\t"
    "s_add_u32 s4, s4, s81\n\t"
    "s_waitcnt vmcnt(14) lgkmcnt(0)\n\t"
    "s_barrier\n\t"
    "v_mfma_f32_16x16x32_bf16 v[128:131], a[88:91], a[36:39], v[128:131]\n\t"
    "ds_read_b64_tr_b16 a[72:73], v24 offset:16384\n\t"
    "v_mfma_f32_16x16x32_bf16 v[144:147], a[88:91], a[40:43], v[144:147]\n\t"
    "ds_read_b128 a[0:3], v16 offset:0\n\t"
    "v_mfma_f32_16x16x32_bf16 v[148:151], a[92:95], a[40:43], v[148:151]\n\t"
    "s_add_u32 m0, s27, 49152\n\t"
    "ds_read_b64_tr_b16 a[74:75], v24 offset:17408\n\t"
    "v_mfma_f32_16x16x32_bf16 v[132:135], a[92:95], a[36:39], v[132:135]\n\t"
    "buffer_load_dwordx4 v8, s[52:55], s74 offen offset:0 lds\n\t"
    "v_mfma_f32_16x16x32_bf16 v[136:139], a[96:99], a[36:39], v[136:139]\n\t"
    "ds_read_b128 a[4:7], v16 offset:2048\n\t"
    "v_mfma_f32_16x16x32_bf16 v[152:155], a[96:99], a[40:43], v[152:155]\n\t"
    "ds_read_b64_tr_b16 a[76:77], v25 offset:16384\n\t"
    "v_mfma_f32_16x16x32_bf16 v[156:159], a[100:103], a[40:43], v[156:159]\n\t"
    "ds_read_b128 a[8:11], v16 offset:4096\n\t"
    "v_mfma_f32_16x16x32_bf16 v[140:143], a[100:103], a[36:39], v[140:143]\n\t"
    "ds_read_b64_tr_b16 a[78:79], v25 offset:17408\n\t"
    "v_mfma_f32_16x16x32_bf16 v[168:171], a[96:99], a[44:47], v[168:171]\n\t"
    "ds_read_b128 a[12:15], v16 offset:6144\n\t"
    "v_mfma_f32_16x16x32_bf16 v[184:187], a[96:99], a[48:51], v[184:187]\n\t"
    "ds_read_b64_tr_b16 a[80:81], v26 offset:16384\n\t"
    "v_mfma_f32_16x16x32_bf16 v[188:191], a[100:103], a[48:51], v[188:191]\n\t"
    "buffer_load_dwordx4 v9, s[52:55], s74 offen offset:1024 lds\n\t"
    "v_mfma_f32_16x16x32_bf16 v[172:175], a[100:103], a[44:47], v[172:175]\n\t"
    "ds_read_b128 a[16:19], v16 offset:8192\n\t"
    "v_mfma_f32_16x16x32_bf16 v[160:163], a[88:91], a[44:47], v[160:163]\n\t"
    "ds_read_b64_tr_b16 a[82:83], v26 offset:17408\n\t"
    "v_mfma_f32_16x16x32_bf16 v[176:179], a[88:91], a[48:51], v[176:179]\n\t"
    "ds_read_b128 a[20:23], v16 offset:10240\n\t"
    "v_mfma_f32_16x16x32_bf16 v[180:183], a[92:95], a[48:51], v[180:183]\n\t"
    "ds_read_b64_tr_b16 a[84:85], v27 offset:16384\n\t"
    "v_mfma_f32_16x16x32_bf16 v[164:167], a[92:95], a[44:47], v[164:167]\n\t"
    "ds_read_b128 a[24:27], v16 offset:12288\n\t"
    "v_mfma_f32_16x16x32_bf16 v[192:195], a[88:91], a[52:55], v[192:195]\n\t"
    "buffer_load_dwordx4 v10, s[52:55], s74 offen offset:2048 lds\n\t"
    "v_mfma_f32_16x16x32_bf16 v[208:211], a[88:91], a[56:59], v[208:211]\n\t"
    "ds_read_b64_tr_b16 a[86:87], v27 offset:17408\n\t"
    "v_mfma_f32_16x16x32_bf16 v[212:215], a[92:95], a[56:59], v[212:215]\n\t"
    "ds_read_b128 a[28:31], v16 offset:14336\n\t"
    "v_mfma_f32_16x16x32_bf16 v[196:199], a[92:95], a[52:55], v[196:199]\n\t"
    "v_and_b32 v6, 56, v61\n\t"
    "v_mfma_f32_16x16x32_bf16 v[200:203], a[96:99], a[52:55], v[200:203]\n\t"
    "v_add_lshl_u32 v6, v6, s4, 10\n\t"
    "v_mfma_f32_16x16x32_bf16 v[216:219], a[96:99], a[56:59], v[216:219]\n\t"
    "v_and_b32 v7, 7, v61\n\t"
    "v_mfma_f32_16x16x32_bf16 v[220:223], a[100:103], a[56:59], v[220:223]\n\t"
    "v_lshl_or_b32 v41, v7, 4, v6\n\t"
    "v_mfma_f32_16x16x32_bf16 v[204:207], a[100:103], a[52:55], v[204:207]\n\t"
    "buffer_load_dwordx4 v11, s[52:55], s74 offen offset:3072 lds\n\t"
    "v_mfma_f32_16x16x32_bf16 v[232:235], a[96:99], a[60:63], v[232:235]\n\t"
    "v_bfe_u32 v6, v61, 3, 3\n\t"
    "v_mfma_f32_16x16x32_bf16 v[248:251], a[96:99], a[64:67], v[248:251]\n\t"
    "v_xor_b32 v7, v7, v6\n\t"
    "v_mfma_f32_16x16x32_bf16 v[252:255], a[100:103], a[64:67], v[252:255]\n\t"
    "s_lshl_b32 s4, s33, 11\n\t"
    "v_mfma_f32_16x16x32_bf16 v[236:239], a[100:103], a[60:63], v[236:239]\n\t"
    "v_lshl_add_u32 v6, v6, 7, s4\n\t"
    "v_mfma_f32_16x16x32_bf16 v[224:227], a[88:91], a[60:63], v[224:227]\n\t"
    "s_add_u32 m0, s36, 65536\n\t"
    "v_mfma_f32_16x16x32_bf16 v[240:243], a[88:91], a[64:67], v[240:243]\n\t"
    "buffer_load_dwordx4 v64, s[44:47], s66 offen offset:0 lds\n\t"
    "v_mfma_f32_16x16x32_bf16 v[244:247], a[92:95], a[64:67], v[244:247]\n\t"
    "v_lshl_or_b32 v42, v7, 4, v6\n\t"
    "v_mfma_f32_16x16x32_bf16 v[228:231], a[92:95], a[60:63], v[228:231]\n\t"
    "v_lshrrev_b32 v6, 1, v4\n\t"
    "s_waitcnt lgkmcnt(0)\n\t"
    "v_mfma_f32_16x16x32_bf16 v[128:131], a[72:75], a[0:3], v[128:131]\n\t"
    "ds_read_b64_tr_b16 a[88:89], v24 offset:24576\n\t"
    "v_mfma_f32_16x16x32_bf16 v[144:147], a[72:75], a[4:7], v[144:147]\n\t"
    "ds_read_b128 a[36:39], v17 offset:0\n\t"
    "v_mfma_f32_16x16x32_bf16 v[148:151], a[76:79], a[4:7], v[148:151]\n\t"
    "ds_read_b64_tr_b16 a[90:91], v24 offset:25600\n\t"
    "v_mfma_f32_16x16x32_bf16 v[132:135], a[76:79], a[0:3], v[132:135]\n\t"
    "buffer_load_dwordx4 v65, s[44:47], s66 offen offset:1024 lds\n\t"
    "v_mfma_f32_16x16x32_bf16 v[136:139], a[80:83], a[0:3], v[136:139]\n\t"
    "ds_read_b128 a[40:43], v17 offset:2048\n\t"
    "v_mfma_f32_16x16x32_bf16 v[152:155], a[80:83], a[4:7], v[152:155]\n\t"
    "ds_read_b64_tr_b16 a[92:93], v25 offset:24576\n\t"
    "v_mfma_f32_16x16x32_bf16 v[156:159], a[84:87], a[4:7], v[156:159]\n\t"
    "ds_read_b128 a[44:47], v17 offset:4096\n\t"
    "v_mfma_f32_16x16x32_bf16 v[140:143], a[84:87], a[0:3], v[140:143]\n\t"
    "ds_read_b64_tr_b16 a[94:95], v25 offset:25600\n\t"
    "v_mfma_f32_16x16x32_bf16 v[168:171], a[80:83], a[8:11], v[168:171]\n\t"
    "ds_read_b128 a[48:51], v17 offset:6144\n\t"
    "v_mfma_f32_16x16x32_bf16 v[184:187], a[80:83], a[12:15], v[184:187]\n\t"
    "s_add_u32 m0, s37, 65536\n\t"
    "ds_read_b64_tr_b16 a[96:97], v26 offset:24576\n\t"
    "v_mfma_f32_16x16x32_bf16 v[188:191], a[84:87], a[12:15], v[188:191]\n\t"
    "buffer_load_dwordx4 v66, s[44:47], s66 offen offset:2048 lds\n\t"
    "v_mfma_f32_16x16x32_bf16 v[172:175], a[84:87], a[8:11], v[172:175]\n\t"
    "ds_read_b128 a[52:55], v17 offset:8192\n\t"
    "v_mfma_f32_16x16x32_bf16 v[160:163], a[72:75], a[8:11], v[160:163]\n\t"
    "ds_read_b64_tr_b16 a[98:99], v26 offset:25600\n\t"
    "v_mfma_f32_16x16x32_bf16 v[176:179], a[72:75], a[12:15], v[176:179]\n\t"
    "ds_read_b128 a[56:59], v17 offset:10240\n\t"
    "v_mfma_f32_16x16x32_bf16 v[180:183], a[76:79], a[12:15], v[180:183]\n\t"
    "ds_read_b64_tr_b16 a[100:101], v27 offset:24576\n\t"
    "v_mfma_f32_16x16x32_bf16 v[164:167], a[76:79], a[8:11], v[164:167]\n\t"
    "ds_read_b128 a[60:63], v17 offset:12288\n\t"
    "v_mfma_f32_16x16x32_bf16 v[192:195], a[72:75], a[16:19], v[192:195]\n\t"
    "buffer_load_dwordx4 v67, s[44:47], s66 offen offset:3072 lds\n\t"
    "v_mfma_f32_16x16x32_bf16 v[208:211], a[72:75], a[20:23], v[208:211]\n\t"
    "ds_read_b64_tr_b16 a[102:103], v27 offset:25600\n\t"
    "v_mfma_f32_16x16x32_bf16 v[212:215], a[76:79], a[20:23], v[212:215]\n\t"
    "ds_read_b128 a[64:67], v17 offset:14336\n\t"
    "v_mfma_f32_16x16x32_bf16 v[196:199], a[76:79], a[16:19], v[196:199]\n\t"
    "s_lshl_b32 s4, s81, 2\n\t"
    "v_mfma_f32_16x16x32_bf16 v[200:203], a[80:83], a[16:19], v[200:203]\n\t"
    "v_or_b32 v6, s4, v6\n\t"
    "v_mfma_f32_16x16x32_bf16 v[216:219], a[80:83], a[20:23], v[216:219]\n\t"
    "v_xor_b32 v6, v6, v5\n\t"
    "v_mfma_f32_16x16x32_bf16 v[220:223], a[84:87], a[20:23], v[220:223]\n\t"
    "s_add_u32 m0, s38, 65536\n\t"
    "v_mfma_f32_16x16x32_bf16 v[204:207], a[84:87], a[16:19], v[204:207]\n\t"
    "buffer_load_dwordx4 v68, s[44:47], s66 offen offset:0 lds\n\t"
    "v_mfma_f32_16x16x32_bf16 v[232:235], a[80:83], a[24:27], v[232:235]\n\t"
    "v_and_b32 v6, 7, v6\n\t"
    "v_mfma_f32_16x16x32_bf16 v[248:251], a[80:83], a[28:31], v[248:251]\n\t"
    "s_lshl_b32 s4, s80, 12\n\t"
    "v_mfma_f32_16x16x32_bf16 v[252:255], a[84:87], a[28:31], v[252:255]\n\t"
    "v_lshl_add_u32 v7, v5, 7, s4\n\t"
    "v_mfma_f32_16x16x32_bf16 v[236:239], a[84:87], a[24:27], v[236:239]\n\t"
    "v_lshl_or_b32 v7, v6, 4, v7\n\t"
    "v_mfma_f32_16x16x32_bf16 v[224:227], a[72:75], a[24:27], v[224:227]\n\t"
    "v_and_b32 v6, 1, v4\n\t"
    "v_mfma_f32_16x16x32_bf16 v[240:243], a[72:75], a[28:31], v[240:243]\n\t"
    "buffer_load_dwordx4 v69, s[44:47], s66 offen offset:1024 lds\n\t"
    "v_mfma_f32_16x16x32_bf16 v[244:247], a[76:79], a[28:31], v[244:247]\n\t"
    "v_lshl_or_b32 v43, v6, 3, v7\n\t"
    "v_mfma_f32_16x16x32_bf16 v[228:231], a[76:79], a[24:27], v[228:231]\n\t"
    "v_xor_b32 v44, 32, v43\n\t"
    "s_waitcnt vmcnt(12) lgkmcnt(0)\n\t"
    "s_barrier\n\t"
    "v_mfma_f32_16x16x32_bf16 v[128:131], a[88:91], a[36:39], v[128:131]\n\t"
    "ds_read_b64_tr_b16 a[72:73], v24 offset:32768\n\t"
    "v_mfma_f32_16x16x32_bf16 v[144:147], a[88:91], a[40:43], v[144:147]\n\t"
    "ds_read_b128 a[0:3], v16 offset:32768\n\t"
    "v_mfma_f32_16x16x32_bf16 v[148:151], a[92:95], a[40:43], v[148:151]\n\t"
    "s_add_u32 m0, s39, 65536\n\t"
    "ds_read_b64_tr_b16 a[74:75], v24 offset:33792\n\t"
    "v_mfma_f32_16x16x32_bf16 v[132:135], a[92:95], a[36:39], v[132:135]\n\t"
    "buffer_load_dwordx4 v70, s[44:47], s66 offen offset:2048 lds\n\t"
    "v_mfma_f32_16x16x32_bf16 v[136:139], a[96:99], a[36:39], v[136:139]\n\t"
    "ds_read_b128 a[4:7], v16 offset:34816\n\t"
    "v_mfma_f32_16x16x32_bf16 v[152:155], a[96:99], a[40:43], v[152:155]\n\t"
    "ds_read_b64_tr_b16 a[76:77], v25 offset:32768\n\t"
    "v_mfma_f32_16x16x32_bf16 v[156:159], a[100:103], a[40:43], v[156:159]\n\t"
    "ds_read_b128 a[8:11], v16 offset:36864\n\t"
    "v_mfma_f32_16x16x32_bf16 v[140:143], a[100:103], a[36:39], v[140:143]\n\t"
    "ds_read_b64_tr_b16 a[78:79], v25 offset:33792\n\t"
    "v_mfma_f32_16x16x32_bf16 v[168:171], a[96:99], a[44:47], v[168:171]\n\t"
    "ds_read_b128 a[12:15], v16 offset:38912\n\t"
    "v_mfma_f32_16x16x32_bf16 v[184:187], a[96:99], a[48:51], v[184:187]\n\t"
    "ds_read_b64_tr_b16 a[80:81], v26 offset:32768\n\t"
    "v_mfma_f32_16x16x32_bf16 v[188:191], a[100:103], a[48:51], v[188:191]\n\t"
    "buffer_load_dwordx4 v71, s[44:47], s66 offen offset:3072 lds\n\t"
    "v_mfma_f32_16x16x32_bf16 v[172:175], a[100:103], a[44:47], v[172:175]\n\t"
    "ds_read_b128 a[16:19], v16 offset:40960\n\t"
    "v_mfma_f32_16x16x32_bf16 v[160:163], a[88:91], a[44:47], v[160:163]\n\t"
    "ds_read_b64_tr_b16 a[82:83], v26 offset:33792\n\t"
    "v_mfma_f32_16x16x32_bf16 v[176:179], a[88:91], a[48:51], v[176:179]\n\t"
    "ds_read_b128 a[20:23], v16 offset:43008\n\t"
    "v_mfma_f32_16x16x32_bf16 v[180:183], a[92:95], a[48:51], v[180:183]\n\t"
    "ds_read_b64_tr_b16 a[84:85], v27 offset:32768\n\t"
    "v_mfma_f32_16x16x32_bf16 v[164:167], a[92:95], a[44:47], v[164:167]\n\t"
    "s_add_u32 m0, s27, 0\n\t"
    "ds_read_b128 a[24:27], v16 offset:45056\n\t"
    "v_mfma_f32_16x16x32_bf16 v[192:195], a[88:91], a[52:55], v[192:195]\n\t"
    "buffer_load_dwordx4 v12, s[48:51], s74 offen offset:0 lds\n\t"
    "v_mfma_f32_16x16x32_bf16 v[208:211], a[88:91], a[56:59], v[208:211]\n\t"
    "ds_read_b64_tr_b16 a[86:87], v27 offset:33792\n\t"
    "v_mfma_f32_16x16x32_bf16 v[212:215], a[92:95], a[56:59], v[212:215]\n\t"
    "ds_read_b128 a[28:31], v16 offset:47104\n\t"
    "v_mfma_f32_16x16x32_bf16 v[196:199], a[92:95], a[52:55], v[196:199]\n\t"
    "s_lshl_b32 s4, s33, 6\n\t"
    "v_mfma_f32_16x16x32_bf16 v[200:203], a[96:99], a[52:55], v[200:203]\n\t"
    "s_add_u32 s4, s4, 101376\n\t"
    "v_mfma_f32_16x16x32_bf16 v[216:219], a[96:99], a[56:59], v[216:219]\n\t"
    "v_mov_b32 v47, s4\n\t"
    "v_mfma_f32_16x16x32_bf16 v[220:223], a[100:103], a[56:59], v[220:223]\n\t"
    "s_xor_b32 s4, s4, 64\n\t"
    "v_mfma_f32_16x16x32_bf16 v[204:207], a[100:103], a[52:55], v[204:207]\n\t"
    "buffer_load_dwordx4 v13, s[48:51], s74 offen offset:1024 lds\n\t"
    "v_mfma_f32_16x16x32_bf16 v[232:235], a[96:99], a[60:63], v[232:235]\n\t"
    "v_mov_b32 v45, s4\n\t"
    "v_mfma_f32_16x16x32_bf16 v[248:251], a[96:99], a[64:67], v[248:251]\n\t"
    "v_mov_b32 v46, 1\n\t"
    "v_mfma_f32_16x16x32_bf16 v[252:255], a[100:103], a[64:67], v[252:255]\n\t"
    "v_bfe_u32 v7, v61, 2, 2\n\t"
    "v_mfma_f32_16x16x32_bf16 v[236:239], a[100:103], a[60:63], v[236:239]\n\t"
    "v_bfrev_b32 v6, 1\n\t"
    "v_mfma_f32_16x16x32_bf16 v[224:227], a[88:91], a[60:63], v[224:227]\n\t"
    "v_cmp_eq_u32 vcc, 0, v7\n\t"
    "v_mfma_f32_16x16x32_bf16 v[240:243], a[88:91], a[64:67], v[240:243]\n\t"
    "buffer_load_dwordx4 v14, s[48:51], s74 offen offset:2048 lds\n\t"
    "v_mfma_f32_16x16x32_bf16 v[244:247], a[92:95], a[64:67], v[244:247]\n\t"
    "s_nop 0\n\t"
    "v_mfma_f32_16x16x32_bf16 v[228:231], a[92:95], a[60:63], v[228:231]\n\t"
    "s_nop 0\n\t"
    "s_waitcnt lgkmcnt(0)\n\t"
    "v_mfma_f32_16x16x32_bf16 v[128:131], a[72:75], a[0:3], v[128:131]\n\t"
    "ds_read_b64_tr_b16 a[88:89], v24 offset:40960\n\t"
    "v_mfma_f32_16x16x32_bf16 v[144:147], a[72:75], a[4:7], v[144:147]\n\t"
    "v_cndmask_b32 v6, v38, v6, vcc\n\t"
    "v_mfma_f32_16x16x32_bf16 v[148:151], a[76:79], a[4:7], v[148:151]\n\t"
    "ds_read_b128 a[32:35], v20 offset:32768\n\t"
    "v_mfma_f32_16x16x32_bf16 v[132:135], a[76:79], a[0:3], v[132:135]\n\t"
    "buffer_load_dwordx4 v15, s[48:51], s74 offen offset:3072 lds\n\t"
    "v_mfma_f32_16x16x32_bf16 v[136:139], a[80:83], a[0:3], v[136:139]\n\t"
    "ds_read_b64_tr_b16 a[90:91], v24 offset:41984\n\t"
    "v_mfma_f32_16x16x32_bf16 v[152:155], a[80:83], a[4:7], v[152:155]\n\t"
    "v_mfma_f32_16x16x32_bf16 v[156:159], a[84:87], a[4:7], v[156:159]\n\t"
    "ds_read_b64_tr_b16 a[92:93], v25 offset:40960\n\t"
    "v_mfma_f32_16x16x32_bf16 v[140:143], a[84:87], a[0:3], v[140:143]\n\t"
    "v_mfma_f32_16x16x32_bf16 v[168:171], a[80:83], a[8:11], v[168:171]\n\t"
    "ds_read_b64_tr_b16 a[94:95], v25 offset:41984\n\t"
    "v_mfma_f32_16x16x32_bf16 v[184:187], a[80:83], a[12:15], v[184:187]\n\t"
    "s_add_u32 m0, s36, 0\n\t"
    "v_mfma_f32_16x16x32_bf16 v[188:191], a[84:87], a[12:15], v[188:191]\n\t"
    "buffer_load_dwordx4 v64, s[40:43], s67 offen offset:0 lds\n\t"
    "v_mfma_f32_16x16x32_bf16 v[172:175], a[84:87], a[8:11], v[172:175]\n\t"
    "ds_read_b64_tr_b16 a[96:97], v26 offset:40960\n\t"
    "v_mfma_f32_16x16x32_bf16 v[160:163], a[72:75], a[8:11], v[160:163]\n\t"
    "v_mfma_f32_16x16x32_bf16 v[176:179], a[72:75], a[12:15], v[176:179]\n\t"
    "ds_read_b64_tr_b16 a[98:99], v26 offset:41984\n\t"
    "v_mfma_f32_16x16x32_bf16 v[180:183], a[76:79], a[12:15], v[180:183]\n\t"
    "v_mfma_f32_16x16x32_bf16 v[164:167], a[76:79], a[8:11], v[164:167]\n\t"
    "ds_read_b64_tr_b16 a[100:101], v27 offset:40960\n\t"
    "v_mfma_f32_16x16x32_bf16 v[192:195], a[72:75], a[16:19], v[192:195]\n\t"
    "buffer_load_dwordx4 v65, s[40:43], s67 offen offset:1024 lds\n\t"
    "v_mfma_f32_16x16x32_bf16 v[208:211], a[72:75], a[20:23], v[208:211]\n\t"
    "ds_read_b64_tr_b16 a[102:103], v27 offset:41984\n\t"
    "v_mfma_f32_16x16x32_bf16 v[212:215], a[76:79], a[20:23], v[212:215]\n\t"
    "v_mfma_f32_16x16x32_bf16 v[196:199], a[76:79], a[16:19], v[196:199]\n\t"
    "v_mfma_f32_16x16x32_bf16 v[200:203], a[80:83], a[16:19], v[200:203]\n\t"
    "v_mfma_f32_16x16x32_bf16 v[216:219], a[80:83], a[20:23], v[216:219]\n\t"
    "v_mfma_f32_16x16x32_bf16 v[220:223], a[84:87], a[20:23], v[220:223]\n\t"
    "s_add_u32 m0, s37, 0\n\t"
    "v_mfma_f32_16x16x32_bf16 v[204:207], a[84:87], a[16:19], v[204:207]\n\t"
    "buffer_load_dwordx4 v66, s[40:43], s67 offen offset:2048 lds\n\t"
    "v_mfma_f32_16x16x32_bf16 v[232:235], a[80:83], a[24:27], v[232:235]\n\t"
    "v_mfma_f32_16x16x32_bf16 v[248:251], a[80:83], a[28:31], v[248:251]\n\t"
    "v_mfma_f32_16x16x32_bf16 v[252:255], a[84:87], a[28:31], v[252:255]\n\t"
    "v_mfma_f32_16x16x32_bf16 v[236:239], a[84:87], a[24:27], v[236:239]\n\t"
    "v_mfma_f32_16x16x32_bf16 v[224:227], a[72:75], a[24:27], v[224:227]\n\t"
    "v_mfma_f32_16x16x32_bf16 v[240:243], a[72:75], a[28:31], v[240:243]\n\t"
    "buffer_load_dwordx4 v67, s[40:43], s67 offen offset:3072 lds\n\t"
    "v_mfma_f32_16x16x32_bf16 v[244:247], a[76:79], a[28:31], v[244:247]\n\t"
    "v_mfma_f32_16x16x32_bf16 v[228:231], a[76:79], a[24:27], v[228:231]\n\t"
    "s_waitcnt vmcnt(16) lgkmcnt(0)\n\t"
    "s_barrier\n\t"
    "v_mfma_f32_16x16x32_bf16 v[128:131], a[88:91], a[4:7], v[128:131]\n\t"
    "ds_read_b64_tr_b16 a[72:73], v24 offset:49152\n\t"
    "v_mfma_f32_16x16x32_bf16 v[144:147], a[88:91], a[8:11], v[144:147]\n\t"
    "ds_read_b128 a[36:39], v17 offset:32768\n\t"
    "v_mfma_f32_16x16x32_bf16 v[148:151], a[92:95], a[8:11], v[148:151]\n\t"
    "s_add_u32 m0, s38, 0\n\t"
    "ds_read_b64_tr_b16 a[74:75], v24 offset:50176\n\t"
    "v_mfma_f32_16x16x32_bf16 v[132:135], a[92:95], a[4:7], v[132:135]\n\t"
    "buffer_load_dwordx4 v68, s[40:43], s67 offen offset:0 lds\n\t"
    "v_mfma_f32_16x16x32_bf16 v[136:139], a[96:99], a[4:7], v[136:139]\n\t"
    "ds_read_b128 a[40:43], v17 offset:34816\n\t"
    "v_mfma_f32_16x16x32_bf16 v[152:155], a[96:99], a[8:11], v[152:155]\n\t"
    "ds_read_b64_tr_b16 a[76:77], v25 offset:49152\n\t"
    "v_mfma_f32_16x16x32_bf16 v[156:159], a[100:103], a[8:11], v[156:159]\n\t"
    "ds_read_b128 a[44:47], v17 offset:36864\n\t"
    "v_mfma_f32_16x16x32_bf16 v[140:143], a[100:103], a[4:7], v[140:143]\n\t"
    "ds_read_b64_tr_b16 a[78:79], v25 offset:50176\n\t"
    "v_mfma_f32_16x16x32_bf16 v[168:171], a[96:99], a[12:15], v[168:171]\n\t"
    "buffer_load_dwordx4 v69, s[40:43], s67 offen offset:1024 lds\n\t"
    "v_mfma_f32_16x16x32_bf16 v[184:187], a[96:99], a[16:19], v[184:187]\n\t"
    "ds_read_b128 a[48:51], v17 offset:38912\n\t"
    "v_mfma_f32_16x16x32_bf16 v[188:191], a[100:103], a[16:19], v[188:191]\n\t"
    "ds_read_b64_tr_b16 a[80:81], v26 offset:49152\n\t"
    "v_mfma_f32_16x16x32_bf16 v[172:175], a[100:103], a[12:15], v[172:175]\n\t"
    "ds_read_b128 a[52:55], v17 offset:40960\n\t"
    "v_mfma_f32_16x16x32_bf16 v[160:163], a[88:91], a[12:15], v[160:163]\n\t"
    "s_add_u32 m0, s39, 0\n\t"
    "ds_read_b64_tr_b16 a[82:83], v26 offset:50176\n\t"
    "v_mfma_f32_16x16x32_bf16 v[176:179], a[88:91], a[16:19], v[176:179]\n\t"
    "buffer_load_dwordx4 v70, s[40:43], s67 offen offset:2048 lds\n\t"
    "v_mfma_f32_16x16x32_bf16 v[180:183], a[92:95], a[16:19], v[180:183]\n\t"
    "ds_read_b128 a[56:59], v17 offset:43008\n\t"
    "v_mfma_f32_16x16x32_bf16 v[164:167], a[92:95], a[12:15], v[164:167]\n\t"
    "ds_read_b64_tr_b16 a[84:85], v27 offset:49152\n\t"
    "v_mfma_f32_16x16x32_bf16 v[192:195], a[88:91], a[20:23], v[192:195]\n\t"
    "ds_read_b128 a[60:63], v17 offset:45056\n\t"
    "v_mfma_f32_16x16x32_bf16 v[208:211], a[88:91], a[24:27], v[208:211]\n\t"
    "ds_read_b64_tr_b16 a[86:87], v27 offset:50176\n\t"
    "v_mfma_f32_16x16x32_bf16 v[212:215], a[92:95], a[24:27], v[212:215]\n\t"
    "ds_read_b128 a[64:67], v17 offset:47104\n\t"
    "v_mfma_f32_16x16x32_bf16 v[196:199], a[92:95], a[20:23], v[196:199]\n\t"
    "buffer_load_dwordx4 v71, s[40:43], s67 offen offset:3072 lds\n\t"
    "v_mfma_f32_16x16x32_bf16 v[200:203], a[96:99], a[20:23], v[200:203]\n\t"
    "v_mfma_f32_16x16x32_bf16 v[216:219], a[96:99], a[24:27], v[216:219]\n\t"
    "v_mfma_f32_16x16x32_bf16 v[220:223], a[100:103], a[24:27], v[220:223]\n\t"
    "v_mfma_f32_16x16x32_bf16 v[204:207], a[100:103], a[20:23], v[204:207]\n\t"
    "s_add_u32 m0, s27, 16384\n\t"
    "v_mfma_f32_16x16x32_bf16 v[232:235], a[96:99], a[28:31], v[232:235]\n\t"
    "buffer_load_dwordx4 v8, s[48:51], s75 offen offset:0 lds\n\t"
    "v_mfma_f32_16x16x32_bf16 v[248:251], a[96:99], a[32:35], v[248:251]\n\t"
    "v_mfma_f32_16x16x32_bf16 v[252:255], a[100:103], a[32:35], v[252:255]\n\t"
    "v_mfma_f32_16x16x32_bf16 v[236:239], a[100:103], a[28:31], v[236:239]\n\t"
    "v_mfma_f32_16x16x32_bf16 v[224:227], a[88:91], a[28:31], v[224:227]\n\t"
    "v_mfma_f32_16x16x32_bf16 v[240:243], a[88:91], a[32:35], v[240:243]\n\t"
    "buffer_load_dwordx4 v9, s[48:51], s75 offen offset:1024 lds\n\t"
    "v_mfma_f32_16x16x32_bf16 v[244:247], a[92:95], a[32:35], v[244:247]\n\t"
    "v_mfma_f32_16x16x32_bf16 v[228:231], a[92:95], a[28:31], v[228:231]\n\t"
    "s_waitcnt lgkmcnt(0)\n\t"
    "v_mfma_f32_16x16x32_bf16 v[128:131], a[72:75], a[36:39], v[128:131]\n\t"
    "ds_read_b64_tr_b16 a[88:89], v24 offset:57344\n\t"
    "v_mfma_f32_16x16x32_bf16 v[144:147], a[72:75], a[40:43], v[144:147]\n\t"
    "v_mfma_f32_16x16x32_bf16 v[148:151], a[76:79], a[40:43], v[148:151]\n\t"
    "ds_read_b128 a[68:71], v21 offset:32768\n\t"
    "v_mfma_f32_16x16x32_bf16 v[132:135], a[76:79], a[36:39], v[132:135]\n\t"
    "buffer_load_dwordx4 v10, s[48:51], s75 offen offset:2048 lds\n\t"
    "v_mfma_f32_16x16x32_bf16 v[136:139], a[80:83], a[36:39], v[136:139]\n\t"
    "ds_read_b64_tr_b16 a[90:91], v24 offset:58368\n\t"
    "v_mfma_f32_16x16x32_bf16 v[152:155], a[80:83], a[40:43], v[152:155]\n\t"
    "v_mfma_f32_16x16x32_bf16 v[156:159], a[84:87], a[40:43], v[156:159]\n\t"
    "ds_read_b64_tr_b16 a[92:93], v25 offset:57344\n\t"
    "v_mfma_f32_16x16x32_bf16 v[140:143], a[84:87], a[36:39], v[140:143]\n\t"
    "v_mfma_f32_16x16x32_bf16 v[168:171], a[80:83], a[44:47], v[168:171]\n\t"
    "ds_read_b64_tr_b16 a[94:95], v25 offset:58368\n\t"
    "v_mfma_f32_16x16x32_bf16 v[184:187], a[80:83], a[48:51], v[184:187]\n\t"
    "v_mfma_f32_16x16x32_bf16 v[188:191], a[84:87], a[48:51], v[188:191]\n\t"
    "buffer_load_dwordx4 v11, s[48:51], s75 offen offset:3072 lds\n\t"
    "v_mfma_f32_16x16x32_bf16 v[172:175], a[84:87], a[44:47], v[172:175]\n\t"
    "ds_read_b64_tr_b16 a[96:97], v26 offset:57344\n\t"
    "v_mfma_f32_16x16x32_bf16 v[160:163], a[72:75], a[44:47], v[160:163]\n\t"
    "v_mfma_f32_16x16x32_bf16 v[176:179], a[72:75], a[48:51], v[176:179]\n\t"
    "ds_read_b64_tr_b16 a[98:99], v26 offset:58368\n\t"
    "v_mfma_f32_16x16x32_bf16 v[180:183], a[76:79], a[48:51], v[180:183]\n\t"
    "v_mfma_f32_16x16x32_bf16 v[164:167], a[76:79], a[44:47], v[164:167]\n\t"
    "s_add_u32 m0, s27, 32768\n\t"
    "ds_read_b64_tr_b16 a[100:101], v27 offset:57344\n\t"
    "v_mfma_f32_16x16x32_bf16 v[192:195], a[72:75], a[52:55], v[192:195]\n\t"
    "buffer_load_dwordx4 v8, s[52:55], s75 offen offset:0 lds\n\t"
    "v_mfma_f32_16x16x32_bf16 v[208:211], a[72:75], a[56:59], v[208:211]\n\t"
    "ds_read_b64_tr_b16 a[102:103], v27 offset:58368\n\t"
    "v_mfma_f32_16x16x32_bf16 v[212:215], a[76:79], a[56:59], v[212:215]\n\t"
    "v_mfma_f32_16x16x32_bf16 v[196:199], a[76:79], a[52:55], v[196:199]\n\t"
    "v_mfma_f32_16x16x32_bf16 v[200:203], a[80:83], a[52:55], v[200:203]\n\t"
    "v_mfma_f32_16x16x32_bf16 v[216:219], a[80:83], a[56:59], v[216:219]\n\t"
    "v_mfma_f32_16x16x32_bf16 v[220:223], a[84:87], a[56:59], v[220:223]\n\t"
    "v_mfma_f32_16x16x32_bf16 v[204:207], a[84:87], a[52:55], v[204:207]\n\t"
    "buffer_load_dwordx4 v9, s[52:55], s75 offen offset:1024 lds\n\t"
    "v_mfma_f32_16x16x32_bf16 v[232:235], a[80:83], a[60:63], v[232:235]\n\t"
    "v_mfma_f32_16x16x32_bf16 v[248:251], a[80:83], a[64:67], v[248:251]\n\t"
    "v_mfma_f32_16x16x32_bf16 v[252:255], a[84:87], a[64:67], v[252:255]\n\t"
    "v_mfma_f32_16x16x32_bf16 v[236:239], a[84:87], a[60:63], v[236:239]\n\t"
    "v_mfma_f32_16x16x32_bf16 v[224:227], a[72:75], a[60:63], v[224:227]\n\t"
    "v_lshl_add_u64 v[48:49], v[48:49], 0, 1\n\t"
    "v_mfma_f32_16x16x32_bf16 v[240:243], a[72:75], a[64:67], v[240:243]\n\t"
    "buffer_store_dwordx2 v[48:49], v30, s[60:63], 0 offen sc1\n\t"
    "v_mfma_f32_16x16x32_bf16 v[244:247], a[76:79], a[64:67], v[244:247]\n\t"
    "v_mfma_f32_16x16x32_bf16 v[228:231], a[76:79], a[60:63], v[228:231]\n\t"
    "s_waitcnt vmcnt(15) lgkmcnt(0)\n\t"
    "s_barrier\n\t"
    "v_mfma_f32_16x16x32_bf16 v[128:131], a[88:91], a[40:43], v[128:131]\n\t"
    "ds_read_b64_tr_b16 a[72:73], v24 offset:0\n\t"
    "v_mfma_f32_16x16x32_bf16 v[144:147], a[88:91], a[44:47], v[144:147]\n\t"
    "ds_read_b128 a[0:3], v18 offset:0\n\t"
    "v_mfma_f32_16x16x32_bf16 v[148:151], a[92:95], a[44:47], v[148:151]\n\t"
    "ds_read_b64_tr_b16 a[74:75], v24 offset:1024\n\t"
    "v_mfma_f32_16x16x32_bf16 v[132:135], a[92:95], a[40:43], v[132:135]\n\t"
    "buffer_load_dwordx4 v10, s[52:55], s75 offen offset:2048 lds\n\t"
    "v_mfma_f32_16x16x32_bf16 v[136:139], a[96:99], a[40:43], v[136:139]\n\t"
    "ds_read_b128 a[4:7], v18 offset:2048\n\t"
    "v_mfma_f32_16x16x32_bf16 v[152:155], a[96:99], a[44:47], v[152:155]\n\t"
    "ds_read_b64_tr_b16 a[76:77], v25 offset:0\n\t"
    "v_mfma_f32_16x16x32_bf16 v[156:159], a[100:103], a[44:47], v[156:159]\n\t"
    "ds_read_b128 a[8:11], v18 offset:4096\n\t"
    "v_mfma_f32_16x16x32_bf16 v[140:143], a[100:103], a[40:43], v[140:143]\n\t"
    "ds_read_b64_tr_b16 a[78:79], v25 offset:1024\n\t"
    "v_mfma_f32_16x16x32_bf16 v[168:171], a[96:99], a[48:51], v[168:171]\n\t"
    "ds_read_b128 a[12:15], v18 offset:6144\n\t"
    "v_mfma_f32_16x16x32_bf16 v[184:187], a[96:99], a[52:55], v[184:187]\n\t"
    "ds_read_b64_tr_b16 a[80:81], v26 offset:0\n\t"
    "v_mfma_f32_16x16x32_bf16 v[188:191], a[100:103], a[52:55], v[188:191]\n\t"
    "buffer_load_dwordx4 v11, s[52:55], s75 offen offset:3072 lds\n\t"
    "v_mfma_f32_16x16x32_bf16 v[172:175], a[100:103], a[48:51], v[172:175]\n\t"
    "ds_read_b128 a[16:19], v18 offset:8192\n\t"
    "v_mfma_f32_16x16x32_bf16 v[160:163], a[88:91], a[48:51], v[160:163]\n\t"
    "ds_read_b64_tr_b16 a[82:83], v26 offset:1024\n\t"
    "v_mfma_f32_16x16x32_bf16 v[176:179], a[88:91], a[52:55], v[176:179]\n\t"
    "ds_read_b128 a[20:23], v18 offset:10240\n\t"
    "v_mfma_f32_16x16x32_bf16 v[180:183], a[92:95], a[52:55], v[180:183]\n\t"
    "ds_read_b64_tr_b16 a[84:85], v27 offset:0\n\t"
    "v_mfma_f32_16x16x32_bf16 v[164:167], a[92:95], a[48:51], v[164:167]\n\t"
    "s_add_u32 m0, s36, 32768\n\t"
    "ds_read_b128 a[24:27], v18 offset:12288\n\t"
    "v_mfma_f32_16x16x32_bf16 v[192:195], a[88:91], a[56:59], v[192:195]\n\t"
    "buffer_load_dwordx4 v64, s[44:47], s67 offen offset:0 lds\n\t"
    "v_mfma_f32_16x16x32_bf16 v[208:211], a[88:91], a[60:63], v[208:211]\n\t"
    "ds_read_b64_tr_b16 a[86:87], v27 offset:1024\n\t"
    "v_mfma_f32_16x16x32_bf16 v[212:215], a[92:95], a[60:63], v[212:215]\n\t"
    "ds_read_b128 a[28:31], v18 offset:14336\n\t"
    "v_mfma_f32_16x16x32_bf16 v[196:199], a[92:95], a[56:59], v[196:199]\n\t"
    "v_mfma_f32_16x16x32_bf16 v[200:203], a[96:99], a[56:59], v[200:203]\n\t"
    "v_mfma_f32_16x16x32_bf16 v[216:219], a[96:99], a[60:63], v[216:219]\n\t"
    "v_mfma_f32_16x16x32_bf16 v[220:223], a[100:103], a[60:63], v[220:223]\n\t"
    "v_mfma_f32_16x16x32_bf16 v[204:207], a[100:103], a[56:59], v[204:207]\n\t"
    "buffer_load_dwordx4 v65, s[44:47], s67 offen offset:1024 lds\n\t"
    "v_mfma_f32_16x16x32_bf16 v[232:235], a[96:99], a[64:67], v[232:235]\n\t"
    "v_mfma_f32_16x16x32_bf16 v[248:251], a[96:99], a[68:71], v[248:251]\n\t"
    "v_mfma_f32_16x16x32_bf16 v[252:255], a[100:103], a[68:71], v[252:255]\n\t"
    "v_mfma_f32_16x16x32_bf16 v[236:239], a[100:103], a[64:67], v[236:239]\n\t"
    "v_mfma_f32_16x16x32_bf16 v[224:227], a[88:91], a[64:67], v[224:227]\n\t"
    "s_add_u32 m0, s37, 32768\n\t"
    "v_mfma_f32_16x16x32_bf16 v[240:243], a[88:91], a[68:71], v[240:243]\n\t"
    "buffer_load_dwordx4 v66, s[44:47], s67 offen offset:2048 lds\n\t"
    "v_mfma_f32_16x16x32_bf16 v[244:247], a[92:95], a[68:71], v[244:247]\n\t"
    "v_mfma_f32_16x16x32_bf16 v[228:231], a[92:95], a[64:67], v[228:231]\n\t"
    "s_waitcnt lgkmcnt(0)\n\t"
    "v_mfma_f32_16x16x32_bf16 v[128:131], a[72:75], a[0:3], v[128:131]\n\t"
    "ds_read_b64_tr_b16 a[88:89], v24 offset:8192\n\t"
    "v_mfma_f32_16x16x32_bf16 v[144:147], a[72:75], a[4:7], v[144:147]\n\t"
    "ds_read_b128 a[36:39], v19 offset:0\n\t"
    "v_mfma_f32_16x16x32_bf16 v[148:151], a[76:79], a[4:7], v[148:151]\n\t"
    "ds_read_b64_tr_b16 a[90:91], v24 offset:9216\n\t"
    "v_mfma_f32_16x16x32_bf16 v[132:135], a[76:79], a[0:3], v[132:135]\n\t"
    "buffer_load_dwordx4 v67, s[44:47], s67 offen offset:3072 lds\n\t"
    "v_mfma_f32_16x16x32_bf16 v[136:139], a[80:83], a[0:3], v[136:139]\n\t"
    "ds_read_b128 a[40:43], v19 offset:2048\n\t"
    "v_mfma_f32_16x16x32_bf16 v[152:155], a[80:83], a[4:7], v[152:155]\n\t"
    "ds_read_b64_tr_b16 a[92:93], v25 offset:8192\n\t"
    "v_mfma_f32_16x16x32_bf16 v[156:159], a[84:87], a[4:7], v[156:159]\n\t"
    "ds_read_b128 a[44:47], v19 offset:4096\n\t"
    "v_mfma_f32_16x16x32_bf16 v[140:143], a[84:87], a[0:3], v[140:143]\n\t"
    "ds_read_b64_tr_b16 a[94:95], v25 offset:9216\n\t"
    "v_mfma_f32_16x16x32_bf16 v[168:171], a[80:83], a[8:11], v[168:171]\n\t"
    "ds_read_b128 a[48:51], v19 offset:6144\n\t"
    "v_mfma_f32_16x16x32_bf16 v[184:187], a[80:83], a[12:15], v[184:187]\n\t"
    "s_add_u32 m0, s38, 32768\n\t"
    "ds_read_b64_tr_b16 a[96:97], v26 offset:8192\n\t"
    "v_mfma_f32_16x16x32_bf16 v[188:191], a[84:87], a[12:15], v[188:191]\n\t"
    "buffer_load_dwordx4 v68, s[44:47], s67 offen offset:0 lds\n\t"
    "v_mfma_f32_16x16x32_bf16 v[172:175], a[84:87], a[8:11], v[172:175]\n\t"
    "ds_read_b128 a[52:55], v19 offset:8192\n\t"
    "v_mfma_f32_16x16x32_bf16 v[160:163], a[72:75], a[8:11], v[160:163]\n\t"
    "ds_read_b64_tr_b16 a[98:99], v26 offset:9216\n\t"
    "v_mfma_f32_16x16x32_bf16 v[176:179], a[72:75], a[12:15], v[176:179]\n\t"
    "ds_read_b128 a[56:59], v19 offset:10240\n\t"
    "v_mfma_f32_16x16x32_bf16 v[180:183], a[76:79], a[12:15], v[180:183]\n\t"
    "ds_read_b64_tr_b16 a[100:101], v27 offset:8192\n\t"
    "v_mfma_f32_16x16x32_bf16 v[164:167], a[76:79], a[8:11], v[164:167]\n\t"
    "ds_read_b128 a[60:63], v19 offset:12288\n\t"
    "v_mfma_f32_16x16x32_bf16 v[192:195], a[72:75], a[16:19], v[192:195]\n\t"
    "buffer_load_dwordx4 v69, s[44:47], s67 offen offset:1024 lds\n\t"
    "v_mfma_f32_16x16x32_bf16 v[208:211], a[72:75], a[20:23], v[208:211]\n\t"
    "ds_read_b64_tr_b16 a[102:103], v27 offset:9216\n\t"
    "v_mfma_f32_16x16x32_bf16 v[212:215], a[76:79], a[20:23], v[212:215]\n\t"
    "ds_read_b128 a[64:67], v19 offset:14336\n\t"
    "v_mfma_f32_16x16x32_bf16 v[196:199], a[76:79], a[16:19], v[196:199]\n\t"
    "v_mfma_f32_16x16x32_bf16 v[200:203], a[80:83], a[16:19], v[200:203]\n\t"
    "v_mfma_f32_16x16x32_bf16 v[216:219], a[80:83], a[20:23], v[216:219]\n\t"
    "v_mfma_f32_16x16x32_bf16 v[220:223], a[84:87], a[20:23], v[220:223]\n\t"
    "s_add_u32 m0, s39, 32768\n\t"
    "v_mfma_f32_16x16x32_bf16 v[204:207], a[84:87], a[16:19], v[204:207]\n\t"
    "buffer_load_dwordx4 v70, s[44:47], s67 offen offset:2048 lds\n\t"
    "v_mfma_f32_16x16x32_bf16 v[232:235], a[80:83], a[24:27], v[232:235]\n\t"
    "v_mfma_f32_16x16x32_bf16 v[248:251], a[80:83], a[28:31], v[248:251]\n\t"
    "v_mfma_f32_16x16x32_bf16 v[252:255], a[84:87], a[28:31], v[252:255]\n\t"
    "v_mfma_f32_16x16x32_bf16 v[236:239], a[84:87], a[24:27], v[236:239]\n\t"
    "v_mfma_f32_16x16x32_bf16 v[224:227], a[72:75], a[24:27], v[224:227]\n\t"
    "v_mfma_f32_16x16x32_bf16 v[240:243], a[72:75], a[28:31], v[240:243]\n\t"
    "buffer_load_dwordx4 v71, s[44:47], s67 offen offset:3072 lds\n\t"
    "v_mfma_f32_16x16x32_bf16 v[244:247], a[76:79], a[28:31], v[244:247]\n\t"
    "v_mfma_f32_16x16x32_bf16 v[228:231], a[76:79], a[24:27], v[228:231]\n\t"
    "s_waitcnt vmcnt(13) lgkmcnt(0)\n\t"
    "s_barrier\n\t"
    "v_mfma_f32_16x16x32_bf16 v[128:131], a[88:91], a[36:39], v[128:131]\n\t"
    "ds_read_b64_tr_b16 a[72:73], v24 offset:16384\n\t"
    "v_mfma_f32_16x16x32_bf16 v[144:147], a[88:91], a[40:43], v[144:147]\n\t"
    "ds_read_b128 a[0:3], v16 offset:0\n\t"
    "v_mfma_f32_16x16x32_bf16 v[148:151], a[92:95], a[40:43], v[148:151]\n\t"
    "s_add_u32 m0, s27, 49152\n\t"
    "ds_read_b64_tr_b16 a[74:75], v24 offset:17408\n\t"
    "v_mfma_f32_16x16x32_bf16 v[132:135], a[92:95], a[36:39], v[132:135]\n\t"
    "buffer_load_dwordx4 v12, s[48:51], s75 offen offset:0 lds\n\t"
    "v_mfma_f32_16x16x32_bf16 v[136:139], a[96:99], a[36:39], v[136:139]\n\t"
    "ds_read_b128 a[4:7], v16 offset:2048\n\t"
    "v_mfma_f32_16x16x32_bf16 v[152:155], a[96:99], a[40:43], v[152:155]\n\t"
    "ds_read_b64_tr_b16 a[76:77], v25 offset:16384\n\t"
    "v_mfma_f32_16x16x32_bf16 v[156:159], a[100:103], a[40:43], v[156:159]\n\t"
    "ds_read_b128 a[8:11], v16 offset:4096\n\t"
    "v_mfma_f32_16x16x32_bf16 v[140:143], a[100:103], a[36:39], v[140:143]\n\t"
    "ds_read_b64_tr_b16 a[78:79], v25 offset:17408\n\t"
    "v_mfma_f32_16x16x32_bf16 v[168:171], a[96:99], a[44:47], v[168:171]\n\t"
    "ds_read_b128 a[12:15], v16 offset:6144\n\t"
    "v_mfma_f32_16x16x32_bf16 v[184:187], a[96:99], a[48:51], v[184:187]\n\t"
    "ds_read_b64_tr_b16 a[80:81], v26 offset:16384\n\t"
    "v_mfma_f32_16x16x32_bf16 v[188:191], a[100:103], a[48:51], v[188:191]\n\t"
    "buffer_load_dwordx4 v13, s[48:51], s75 offen offset:1024 lds\n\t"
    "v_mfma_f32_16x16x32_bf16 v[172:175], a[100:103], a[44:47], v[172:175]\n\t"
    "ds_read_b128 a[16:19], v16 offset:8192\n\t"
    "v_mfma_f32_16x16x32_bf16 v[160:163], a[88:91], a[44:47], v[160:163]\n\t"
    "ds_read_b64_tr_b16 a[82:83], v26 offset:17408\n\t"
    "v_mfma_f32_16x16x32_bf16 v[176:179], a[88:91], a[48:51], v[176:179]\n\t"
    "ds_read_b128 a[20:23], v16 offset:10240\n\t"
    "v_mfma_f32_16x16x32_bf16 v[180:183], a[92:95], a[48:51], v[180:183]\n\t"
    "ds_read_b64_tr_b16 a[84:85], v27 offset:16384\n\t"
    "v_mfma_f32_16x16x32_bf16 v[164:167], a[92:95], a[44:47], v[164:167]\n\t"
    "ds_read_b128 a[24:27], v16 offset:12288\n\t"
    "v_mfma_f32_16x16x32_bf16 v[192:195], a[88:91], a[52:55], v[192:195]\n\t"
    "buffer_load_dwordx4 v14, s[48:51], s75 offen offset:2048 lds\n\t"
    "v_mfma_f32_16x16x32_bf16 v[208:211], a[88:91], a[56:59], v[208:211]\n\t"
    "ds_read_b64_tr_b16 a[86:87], v27 offset:17408\n\t"
    "v_mfma_f32_16x16x32_bf16 v[212:215], a[92:95], a[56:59], v[212:215]\n\t"
    "ds_read_b128 a[28:31], v16 offset:14336\n\t"
    "v_mfma_f32_16x16x32_bf16 v[196:199], a[92:95], a[52:55], v[196:199]\n\t"
    "v_mfma_f32_16x16x32_bf16 v[200:203], a[96:99], a[52:55], v[200:203]\n\t"
    "v_mfma_f32_16x16x32_bf16 v[216:219], a[96:99], a[56:59], v[216:219]\n\t"
    "v_mfma_f32_16x16x32_bf16 v[220:223], a[100:103], a[56:59], v[220:223]\n\t"
    "v_mfma_f32_16x16x32_bf16 v[204:207], a[100:103], a[52:55], v[204:207]\n\t"
    "buffer_load_dwordx4 v15, s[48:51], s75 offen offset:3072 lds\n\t"
    "v_mfma_f32_16x16x32_bf16 v[232:235], a[96:99], a[60:63], v[232:235]\n\t"
    "v_mfma_f32_16x16x32_bf16 v[248:251], a[96:99], a[64:67], v[248:251]\n\t"
    "v_mfma_f32_16x16x32_bf16 v[252:255], a[100:103], a[64:67], v[252:255]\n\t"
    "v_mfma_f32_16x16x32_bf16 v[236:239], a[100:103], a[60:63], v[236:239]\n\t"
    "v_mfma_f32_16x16x32_bf16 v[224:227], a[88:91], a[60:63], v[224:227]\n\t"
    "s_add_u32 m0, s36, 65536\n\t"
    "v_mfma_f32_16x16x32_bf16 v[240:243], a[88:91], a[64:67], v[240:243]\n\t"
    "buffer_load_dwordx4 v64, s[40:43], s68 offen offset:0 lds\n\t"
    "v_mfma_f32_16x16x32_bf16 v[244:247], a[92:95], a[64:67], v[244:247]\n\t"
    "v_mfma_f32_16x16x32_bf16 v[228:231], a[92:95], a[60:63], v[228:231]\n\t"
    "s_waitcnt lgkmcnt(0)\n\t"
    "v_mfma_f32_16x16x32_bf16 v[128:131], a[72:75], a[0:3], v[128:131]\n\t"
    "ds_read_b64_tr_b16 a[88:89], v24 offset:24576\n\t"
    "v_mfma_f32_16x16x32_bf16 v[144:147], a[72:75], a[4:7], v[144:147]\n\t"
    "v_mfma_f32_16x16x32_bf16 v[148:151], a[76:79], a[4:7], v[148:151]\n\t"
    "ds_read_b128 a[32:35], v20 offset:0\n\t"
    "v_mfma_f32_16x16x32_bf16 v[132:135], a[76:79], a[0:3], v[132:135]\n\t"
    "buffer_load_dwordx4 v65, s[40:43], s68 offen offset:1024 lds\n\t"
    "v_mfma_f32_16x16x32_bf16 v[136:139], a[80:83], a[0:3], v[136:139]\n\t"
    "ds_read_b64_tr_b16 a[90:91], v24 offset:25600\n\t"
    "v_mfma_f32_16x16x32_bf16 v[152:155], a[80:83], a[4:7], v[152:155]\n\t"
    "v_mfma_f32_16x16x32_bf16 v[156:159], a[84:87], a[4:7], v[156:159]\n\t"
    "ds_read_b64_tr_b16 a[92:93], v25 offset:24576\n\t"
    "v_mfma_f32_16x16x32_bf16 v[140:143], a[84:87], a[0:3], v[140:143]\n\t"
    "v_mfma_f32_16x16x32_bf16 v[168:171], a[80:83], a[8:11], v[168:171]\n\t"
    "ds_read_b64_tr_b16 a[94:95], v25 offset:25600\n\t"
    "v_mfma_f32_16x16x32_bf16 v[184:187], a[80:83], a[12:15], v[184:187]\n\t"
    "s_add_u32 m0, s37, 65536\n\t"
    "v_mfma_f32_16x16x32_bf16 v[188:191], a[84:87], a[12:15], v[188:191]\n\t"
    "buffer_load_dwordx4 v66, s[40:43], s68 offen offset:2048 lds\n\t"
    "v_mfma_f32_16x16x32_bf16 v[172:175], a[84:87], a[8:11], v[172:175]\n\t"
    "ds_read_b64_tr_b16 a[96:97], v26 offset:24576\n\t"
    "v_mfma_f32_16x16x32_bf16 v[160:163], a[72:75], a[8:11], v[160:163]\n\t"
    "v_mfma_f32_16x16x32_bf16 v[176:179], a[72:75], a[12:15], v[176:179]\n\t"
    "ds_read_b64_tr_b16 a[98:99], v26 offset:25600\n\t"
    "v_mfma_f32_16x16x32_bf16 v[180:183], a[76:79], a[12:15], v[180:183]\n\t"
    "v_mfma_f32_16x16x32_bf16 v[164:167], a[76:79], a[8:11], v[164:167]\n\t"
    "ds_read_b64_tr_b16 a[100:101], v27 offset:24576\n\t"
    "v_mfma_f32_16x16x32_bf16 v[192:195], a[72:75], a[16:19], v[192:195]\n\t"
    "buffer_load_dwordx4 v67, s[40:43], s68 offen offset:3072 lds\n\t"
    "v_mfma_f32_16x16x32_bf16 v[208:211], a[72:75], a[20:23], v[208:211]\n\t"
    "ds_read_b64_tr_b16 a[102:103], v27 offset:25600\n\t"
    "v_mfma_f32_16x16x32_bf16 v[212:215], a[76:79], a[20:23], v[212:215]\n\t"
    "v_mfma_f32_16x16x32_bf16 v[196:199], a[76:79], a[16:19], v[196:199]\n\t"
    "v_mfma_f32_16x16x32_bf16 v[200:203], a[80:83], a[16:19], v[200:203]\n\t"
    "v_mfma_f32_16x16x32_bf16 v[216:219], a[80:83], a[20:23], v[216:219]\n\t"
    "v_mfma_f32_16x16x32_bf16 v[220:223], a[84:87], a[20:23], v[220:223]\n\t"
    "s_add_u32 m0, s38, 65536\n\t"
    "v_mfma_f32_16x16x32_bf16 v[204:207], a[84:87], a[16:19], v[204:207]\n\t"
    "buffer_load_dwordx4 v68, s[40:43], s68 offen offset:0 lds\n\t"
    "v_mfma_f32_16x16x32_bf16 v[232:235], a[80:83], a[24:27], v[232:235]\n\t"
    "v_mfma_f32_16x16x32_bf16 v[248:251], a[80:83], a[28:31], v[248:251]\n\t"
    "v_mfma_f32_16x16x32_bf16 v[252:255], a[84:87], a[28:31], v[252:255]\n\t"
    "v_mfma_f32_16x16x32_bf16 v[236:239], a[84:87], a[24:27], v[236:239]\n\t"
    "v_mfma_f32_16x16x32_bf16 v[224:227], a[72:75], a[24:27], v[224:227]\n\t"
    "v_mfma_f32_16x16x32_bf16 v[240:243], a[72:75], a[28:31], v[240:243]\n\t"
    "buffer_load_dwordx4 v69, s[40:43], s68 offen offset:1024 lds\n\t"
    "v_mfma_f32_16x16x32_bf16 v[244:247], a[76:79], a[28:31], v[244:247]\n\t"
    "v_mfma_f32_16x16x32_bf16 v[228:231], a[76:79], a[24:27], v[228:231]\n\t"
    "s_waitcnt vmcnt(18) lgkmcnt(0)\n\t"
    "s_barrier\n\t"
    "v_mfma_f32_16x16x32_bf16 v[128:131], a[88:91], a[4:7], v[128:131]\n\t"
    "ds_read_b64_tr_b16 a[72:73], v24 offset:32768\n\t"
    "v_mfma_f32_16x16x32_bf16 v[144:147], a[88:91], a[8:11], v[144:147]\n\t"
    "s_add_u32 m0, s39, 65536\n\t"
    "ds_read_b128 a[36:39], v17 offset:0\n\t"
    "v_mfma_f32_16x16x32_bf16 v[148:151], a[92:95], a[8:11], v[148:151]\n\t"
    "buffer_load_dwordx4 v70, s[40:43], s68 offen offset:2048 lds\n\t"
    "v_mfma_f32_16x16x32_bf16 v[132:135], a[92:95], a[4:7], v[132:135]\n\t"
    "ds_read_b64_tr_b16 a[74:75], v24 offset:33792\n\t"
    "v_mfma_f32_16x16x32_bf16 v[136:139], a[96:99], a[4:7], v[136:139]\n\t"
    "ds_read_b128 a[40:43], v17 offset:2048\n\t"
    "v_mfma_f32_16x16x32_bf16 v[152:155], a[96:99], a[8:11], v[152:155]\n\t"
    "ds_read_b64_tr_b16 a[76:77], v25 offset:32768\n\t"
    "v_mfma_f32_16x16x32_bf16 v[156:159], a[100:103], a[8:11], v[156:159]\n\t"
    "ds_read_b128 a[44:47], v17 offset:4096\n\t"
    "v_mfma_f32_16x16x32_bf16 v[140:143], a[100:103], a[4:7], v[140:143]\n\t"
    "buffer_load_dwordx4 v71, s[40:43], s68 offen offset:3072 lds\n\t"
    "v_mfma_f32_16x16x32_bf16 v[168:171], a[96:99], a[12:15], v[168:171]\n\t"
    "ds_read_b64_tr_b16 a[78:79], v25 offset:33792\n\t"
    "v_mfma_f32_16x16x32_bf16 v[184:187], a[96:99], a[16:19], v[184:187]\n\t"
    "ds_read_b128 a[48:51], v17 offset:6144\n\t"
    "v_mfma_f32_16x16x32_bf16 v[188:191], a[100:103], a[16:19], v[188:191]\n\t"
    "s_add_u32 m0, s27, 0\n\t"
    "ds_read_b64_tr_b16 a[80:81], v26 offset:32768\n\t"
    "v_mfma_f32_16x16x32_bf16 v[172:175], a[100:103], a[12:15], v[172:175]\n\t"
    "buffer_load_dwordx4 v8, s[48:51], s76 offen offset:0 lds\n\t"
    "v_mfma_f32_16x16x32_bf16 v[160:163], a[88:91], a[12:15], v[160:163]\n\t"
    "ds_read_b128 a[52:55], v17 offset:8192\n\t"
    "v_mfma_f32_16x16x32_bf16 v[176:179], a[88:91], a[16:19], v[176:179]\n\t"
    "ds_read_b64_tr_b16 a[82:83], v26 offset:33792\n\t"
    "v_mfma_f32_16x16x32_bf16 v[180:183], a[92:95], a[16:19], v[180:183]\n\t"
    "ds_read_b128 a[56:59], v17 offset:10240\n\t"
    "v_mfma_f32_16x16x32_bf16 v[164:167], a[92:95], a[12:15], v[164:167]\n\t"
    "ds_read_b64_tr_b16 a[84:85], v27 offset:32768\n\t"
    "v_mfma_f32_16x16x32_bf16 v[192:195], a[88:91], a[20:23], v[192:195]\n\t"
    "buffer_load_dwordx4 v9, s[48:51], s76 offen offset:1024 lds\n\t"
    "v_mfma_f32_16x16x32_bf16 v[208:211], a[88:91], a[24:27], v[208:211]\n\t"
    "ds_read_b128 a[60:63], v17 offset:12288\n\t"
    "v_mfma_f32_16x16x32_bf16 v[212:215], a[92:95], a[24:27], v[212:215]\n\t"
    "ds_read_b64_tr_b16 a[86:87], v27 offset:33792\n\t"
    "v_mfma_f32_16x16x32_bf16 v[196:199], a[92:95], a[20:23], v[196:199]\n\t"
    "ds_read_b128 a[64:67], v17 offset:14336\n\t"
    "v_mfma_f32_16x16x32_bf16 v[200:203], a[96:99], a[20:23], v[200:203]\n\t"
    "v_mfma_f32_16x16x32_bf16 v[216:219], a[96:99], a[24:27], v[216:219]\n\t"
    "buffer_load_dwordx4 v10, s[48:51], s76 offen offset:2048 lds\n\t"
    "v_mfma_f32_16x16x32_bf16 v[220:223], a[100:103], a[24:27], v[220:223]\n\t"
    "v_mfma_f32_16x16x32_bf16 v[204:207], a[100:103], a[20:23], v[204:207]\n\t"
    "v_mfma_f32_16x16x32_bf16 v[232:235], a[96:99], a[28:31], v[232:235]\n\t"
    "v_mfma_f32_16x16x32_bf16 v[248:251], a[96:99], a[32:35], v[248:251]\n\t"
    "buffer_load_dwordx4 v11, s[48:51], s76 offen offset:3072 lds\n\t"
    "v_mfma_f32_16x16x32_bf16 v[252:255], a[100:103], a[32:35], v[252:255]\n\t"
    "v_mfma_f32_16x16x32_bf16 v[236:239], a[100:103], a[28:31], v[236:239]\n\t"
    "v_mfma_f32_16x16x32_bf16 v[224:227], a[88:91], a[28:31], v[224:227]\n\t"
    "v_mfma_f32_16x16x32_bf16 v[240:243], a[88:91], a[32:35], v[240:243]\n\t"
    "s_add_u32 m0, s27, 16384\n\t"
    "v_mfma_f32_16x16x32_bf16 v[244:247], a[92:95], a[32:35], v[244:247]\n\t"
    "buffer_load_dwordx4 v8, s[52:55], s76 offen offset:0 lds\n\t"
    "v_mfma_f32_16x16x32_bf16 v[228:231], a[92:95], a[28:31], v[228:231]\n\t"
    "s_waitcnt lgkmcnt(0)\n\t"
    "v_mfma_f32_16x16x32_bf16 v[128:131], a[72:75], a[36:39], v[128:131]\n\t"
    "ds_read_b64_tr_b16 a[88:89], v24 offset:40960\n\t"
    "v_mfma_f32_16x16x32_bf16 v[144:147], a[72:75], a[40:43], v[144:147]\n\t"
    "v_mfma_f32_16x16x32_bf16 v[148:151], a[76:79], a[40:43], v[148:151]\n\t"
    "buffer_load_dwordx4 v9, s[52:55], s76 offen offset:1024 lds\n\t"
    "v_mfma_f32_16x16x32_bf16 v[132:135], a[76:79], a[36:39], v[132:135]\n\t"
    "ds_read_b128 a[68:71], v21 offset:0\n\t"
    "v_mfma_f32_16x16x32_bf16 v[136:139], a[80:83], a[36:39], v[136:139]\n\t"
    "v_mfma_f32_16x16x32_bf16 v[152:155], a[80:83], a[40:43], v[152:155]\n\t"
    "ds_read_b64_tr_b16 a[90:91], v24 offset:41984\n\t"
    "v_mfma_f32_16x16x32_bf16 v[156:159], a[84:87], a[40:43], v[156:159]\n\t"
    "v_mfma_f32_16x16x32_bf16 v[140:143], a[84:87], a[36:39], v[140:143]\n\t"
    "buffer_load_dwordx4 v10, s[52:55], s76 offen offset:2048 lds\n\t"
    "v_mfma_f32_16x16x32_bf16 v[168:171], a[80:83], a[44:47], v[168:171]\n\t"
    "ds_read_b64_tr_b16 a[92:93], v25 offset:40960\n\t"
    "v_mfma_f32_16x16x32_bf16 v[184:187], a[80:83], a[48:51], v[184:187]\n\t"
    "v_mfma_f32_16x16x32_bf16 v[188:191], a[84:87], a[48:51], v[188:191]\n\t"
    "ds_read_b64_tr_b16 a[94:95], v25 offset:41984\n\t"
    "v_mfma_f32_16x16x32_bf16 v[172:175], a[84:87], a[44:47], v[172:175]\n\t"
    "buffer_load_dwordx4 v11, s[52:55], s76 offen offset:3072 lds\n\t"
    "v_mfma_f32_16x16x32_bf16 v[160:163], a[72:75], a[44:47], v[160:163]\n\t"
    "ds_read_b64_tr_b16 a[96:97], v26 offset:40960\n\t"
    "v_mfma_f32_16x16x32_bf16 v[176:179], a[72:75], a[48:51], v[176:179]\n\t"
    "v_mfma_f32_16x16x32_bf16 v[180:183], a[76:79], a[48:51], v[180:183]\n\t"
    "ds_read_b64_tr_b16 a[98:99], v26 offset:41984\n\t"
    "v_mfma_f32_16x16x32_bf16 v[164:167], a[76:79], a[44:47], v[164:167]\n\t"
    "v_mfma_f32_16x16x32_bf16 v[192:195], a[72:75], a[52:55], v[192:195]\n\t"
    "global_load_dword v57, v35, s[12:13]\n\t"
    "v_mfma_f32_16x16x32_bf16 v[208:211], a[72:75], a[56:59], v[208:211]\n\t"
    "ds_read_b64_tr_b16 a[100:101], v27 offset:40960\n\t"
    "v_mfma_f32_16x16x32_bf16 v[212:215], a[76:79], a[56:59], v[212:215]\n\t"
    "v_mfma_f32_16x16x32_bf16 v[196:199], a[76:79], a[52:55], v[196:199]\n\t"
    "ds_read_b64_tr_b16 a[102:103], v27 offset:41984\n\t"
    "v_mfma_f32_16x16x32_bf16 v[200:203], a[80:83], a[52:55], v[200:203]\n\t"
    "v_mfma_f32_16x16x32_bf16 v[216:219], a[80:83], a[56:59], v[216:219]\n\t"
    "global_load_dword v58, v35, s[14:15]\n\t"
    "v_mfma_f32_16x16x32_bf16 v[220:223], a[84:87], a[56:59], v[220:223]\n\t"
    "v_mfma_f32_16x16x32_bf16 v[204:207], a[84:87], a[52:55], v[204:207]\n\t"
    "v_mfma_f32_16x16x32_bf16 v[232:235], a[80:83], a[60:63], v[232:235]\n\t"
    "v_mfma_f32_16x16x32_bf16 v[248:251], a[80:83], a[64:67], v[248:251]\n\t"
    "global_load_dword v108, v35, s[12:13] offset:128\n\t"
    "v_mfma_f32_16x16x32_bf16 v[252:255], a[84:87], a[64:67], v[252:255]\n\t"
    "v_mfma_f32_16x16x32_bf16 v[236:239], a[84:87], a[60:63], v[236:239]\n\t"
    "v_mfma_f32_16x16x32_bf16 v[224:227], a[72:75], a[60:63], v[224:227]\n\t"
    "v_mfma_f32_16x16x32_bf16 v[240:243], a[72:75], a[64:67], v[240:243]\n\t"
    "v_mfma_f32_16x16x32_bf16 v[244:247], a[76:79], a[64:67], v[244:247]\n\t"
    "global_load_dword v109, v35, s[14:15] offset:128\n\t"
    "v_mfma_f32_16x16x32_bf16 v[228:231], a[76:79], a[60:63], v[228:231]\n\t"
    "s_waitcnt vmcnt(20) lgkmcnt(0)\n\t"
    "s_barrier\n\t"
    "v_mfma_f32_16x16x32_bf16 v[128:131], a[88:91], a[40:43], v[128:131]\n\t"
    "ds_read_b64_tr_b16 a[72:73], v24 offset:49152\n\t"
    "v_mfma_f32_16x16x32_bf16 v[144:147], a[88:91], a[44:47], v[144:147]\n\t"
    "ds_read_b128 a[0:3], v16 offset:32768\n\t"
    "v_mfma_f32_16x16x32_bf16 v[148:151], a[92:95], a[44:47], v[148:151]\n\t"
    "s_add_u32 m0, s36, 0\n\t"
    "ds_read_b64_tr_b16 a[74:75], v24 offset:50176\n\t"
    "v_mfma_f32_16x16x32_bf16 v[132:135], a[92:95], a[40:43], v[132:135]\n\t"
    "buffer_load_dwordx4 v64, s[44:47], s68 offen offset:0 lds\n\t"
    "v_mfma_f32_16x16x32_bf16 v[136:139], a[96:99], a[40:43], v[136:139]\n\t"
    "ds_read_b128 a[4:7], v16 offset:34816\n\t"
    "v_mfma_f32_16x16x32_bf16 v[152:155], a[96:99], a[44:47], v[152:155]\n\t"
    "ds_read_b64_tr_b16 a[76:77], v25 offset:49152\n\t"
    "v_mfma_f32_16x16x32_bf16 v[156:159], a[100:103], a[44:47], v[156:159]\n\t"
    "ds_read_b128 a[8:11], v16 offset:36864\n\t"
    "v_mfma_f32_16x16x32_bf16 v[140:143], a[100:103], a[40:43], v[140:143]\n\t"
    "ds_read_b64_tr_b16 a[78:79], v25 offset:50176\n\t"
    "v_mfma_f32_16x16x32_bf16 v[168:171], a[96:99], a[48:51], v[168:171]\n\t"
    "ds_read_b128 a[12:15], v16 offset:38912\n\t"
    "v_mfma_f32_16x16x32_bf16 v[184:187], a[96:99], a[52:55], v[184:187]\n\t"
    "ds_read_b64_tr_b16 a[80:81], v26 offset:49152\n\t"
    "v_mfma_f32_16x16x32_bf16 v[188:191], a[100:103], a[52:55], v[188:191]\n\t"
    "buffer_load_dwordx4 v65, s[44:47], s68 offen offset:1024 lds\n\t"
    "v_mfma_f32_16x16x32_bf16 v[172:175], a[100:103], a[48:51], v[172:175]\n\t"
    "ds_read_b128 a[16:19], v16 offset:40960\n\t"
    "v_mfma_f32_16x16x32_bf16 v[160:163], a[88:91], a[48:51], v[160:163]\n\t"
    "ds_read_b64_tr_b16 a[82:83], v26 offset:50176\n\t"
    "v_mfma_f32_16x16x32_bf16 v[176:179], a[88:91], a[52:55], v[176:179]\n\t"
    "ds_read_b128 a[20:23], v16 offset:43008\n\t"
    "v_mfma_f32_16x16x32_bf16 v[180:183], a[92:95], a[52:55], v[180:183]\n\t"
    "ds_read_b64_tr_b16 a[84:85], v27 offset:49152\n\t"
    "v_mfma_f32_16x16x32_bf16 v[164:167], a[92:95], a[48:51], v[164:167]\n\t"
    "s_add_u32 m0, s37, 0\n\t"
    "ds_read_b128 a[24:27], v16 offset:45056\n\t"
    "v_mfma_f32_16x16x32_bf16 v[192:195], a[88:91], a[56:59], v[192:195]\n\t"
    "buffer_load_dwordx4 v66, s[44:47], s68 offen offset:2048 lds\n\t"
    "v_mfma_f32_16x16x32_bf16 v[208:211], a[88:91], a[60:63], v[208:211]\n\t"
    "ds_read_b64_tr_b16 a[86:87], v27 offset:50176\n\t"
    "v_mfma_f32_16x16x32_bf16 v[212:215], a[92:95], a[60:63], v[212:215]\n\t"
    "ds_read_b128 a[28:31], v16 offset:47104\n\t"
    "v_mfma_f32_16x16x32_bf16 v[196:199], a[92:95], a[56:59], v[196:199]\n\t"
    "v_mfma_f32_16x16x32_bf16 v[200:203], a[96:99], a[56:59], v[200:203]\n\t"
    "v_mfma_f32_16x16x32_bf16 v[216:219], a[96:99], a[60:63], v[216:219]\n\t"
    "v_mfma_f32_16x16x32_bf16 v[220:223], a[100:103], a[60:63], v[220:223]\n\t"
    "v_mfma_f32_16x16x32_bf16 v[204:207], a[100:103], a[56:59], v[204:207]\n\t"
    "buffer_load_dwordx4 v67, s[44:47], s68 offen offset:3072 lds\n\t"
    "v_mfma_f32_16x16x32_bf16 v[232:235], a[96:99], a[64:67], v[232:235]\n\t"
    "v_mfma_f32_16x16x32_bf16 v[248:251], a[96:99], a[68:71], v[248:251]\n\t"
    "v_mfma_f32_16x16x32_bf16 v[252:255], a[100:103], a[68:71], v[252:255]\n\t"
    "v_mfma_f32_16x16x32_bf16 v[236:239], a[100:103], a[64:67], v[236:239]\n\t"
    "v_mfma_f32_16x16x32_bf16 v[224:227], a[88:91], a[64:67], v[224:227]\n\t"
    "s_add_u32 m0, s38, 0\n\t"
    "v_mfma_f32_16x16x32_bf16 v[240:243], a[88:91], a[68:71], v[240:243]\n\t"
    "buffer_load_dwordx4 v68, s[44:47], s68 offen offset:0 lds\n\t"
    "v_mfma_f32_16x16x32_bf16 v[244:247], a[92:95], a[68:71], v[244:247]\n\t"
    "v_mfma_f32_16x16x32_bf16 v[228:231], a[92:95], a[64:67], v[228:231]\n\t"
    "s_waitcnt lgkmcnt(0)\n\t"
    "v_mfma_f32_16x16x32_bf16 v[128:131], a[72:75], a[0:3], v[128:131]\n\t"
    "ds_read_b64_tr_b16 a[88:89], v24 offset:57344\n\t"
    "v_mfma_f32_16x16x32_bf16 v[144:147], a[72:75], a[4:7], v[144:147]\n\t"
    "ds_read_b128 a[36:39], v17 offset:32768\n\t"
    "v_mfma_f32_16x16x32_bf16 v[148:151], a[76:79], a[4:7], v[148:151]\n\t"
    "ds_read_b64_tr_b16 a[90:91], v24 offset:58368\n\t"
    "v_mfma_f32_16x16x32_bf16 v[132:135], a[76:79], a[0:3], v[132:135]\n\t"
    "buffer_load_dwordx4 v69, s[44:47], s68 offen offset:1024 lds\n\t"
    "v_mfma_f32_16x16x32_bf16 v[136:139], a[80:83], a[0:3], v[136:139]\n\t"
    "ds_read_b128 a[40:43], v17 offset:34816\n\t"
    "v_mfma_f32_16x16x32_bf16 v[152:155], a[80:83], a[4:7], v[152:155]\n\t"
    "ds_read_b64_tr_b16 a[92:93], v25 offset:57344\n\t"
    "v_mfma_f32_16x16x32_bf16 v[156:159], a[84:87], a[4:7], v[156:159]\n\t"
    "ds_read_b128 a[44:47], v17 offset:36864\n\t"
    "v_mfma_f32_16x16x32_bf16 v[140:143], a[84:87], a[0:3], v[140:143]\n\t"
    "ds_read_b64_tr_b16 a[94:95], v25 offset:58368\n\t"
    "v_mfma_f32_16x16x32_bf16 v[168:171], a[80:83], a[8:11], v[168:171]\n\t"
    "ds_read_b128 a[48:51], v17 offset:38912\n\t"
    "v_mfma_f32_16x16x32_bf16 v[184:187], a[80:83], a[12:15], v[184:187]\n\t"
    "s_add_u32 m0, s39, 0\n\t"
    "ds_read_b64_tr_b16 a[96:97], v26 offset:57344\n\t"
    "v_mfma_f32_16x16x32_bf16 v[188:191], a[84:87], a[12:15], v[188:191]\n\t"
    "buffer_load_dwordx4 v70, s[44:47], s68 offen offset:2048 lds\n\t"
    "v_mfma_f32_16x16x32_bf16 v[172:175], a[84:87], a[8:11], v[172:175]\n\t"
    "ds_read_b128 a[52:55], v17 offset:40960\n\t"
    "v_mfma_f32_16x16x32_bf16 v[160:163], a[72:75], a[8:11], v[160:163]\n\t"
    "ds_read_b64_tr_b16 a[98:99], v26 offset:58368\n\t"
    "v_mfma_f32_16x16x32_bf16 v[176:179], a[72:75], a[12:15], v[176:179]\n\t"
    "ds_read_b128 a[56:59], v17 offset:43008\n\t"
    "v_mfma_f32_16x16x32_bf16 v[180:183], a[76:79], a[12:15], v[180:183]\n\t"
    "ds_read_b64_tr_b16 a[100:101], v27 offset:57344\n\t"
    "v_mfma_f32_16x16x32_bf16 v[164:167], a[76:79], a[8:11], v[164:167]\n\t"
    "ds_read_b128 a[60:63], v17 offset:45056\n\t"
    "v_mfma_f32_16x16x32_bf16 v[192:195], a[72:75], a[16:19], v[192:195]\n\t"
    "buffer_load_dwordx4 v71, s[44:47], s68 offen offset:3072 lds\n\t"
    "v_mfma_f32_16x16x32_bf16 v[208:211], a[72:75], a[20:23], v[208:211]\n\t"
    "ds_read_b64_tr_b16 a[102:103], v27 offset:58368\n\t"
    "v_mfma_f32_16x16x32_bf16 v[212:215], a[76:79], a[20:23], v[212:215]\n\t"
    "ds_read_b128 a[64:67], v17 offset:47104\n\t"
    "v_mfma_f32_16x16x32_bf16 v[196:199], a[76:79], a[16:19], v[196:199]\n\t"
    "v_mfma_f32_16x16x32_bf16 v[200:203], a[80:83], a[16:19], v[200:203]\n\t"
    "v_mfma_f32_16x16x32_bf16 v[216:219], a[80:83], a[20:23], v[216:219]\n\t"
    "v_mfma_f32_16x16x32_bf16 v[220:223], a[84:87], a[20:23], v[220:223]\n\t"
    "s_add_u32 m0, s27, 32768\n\t"
    "v_mfma_f32_16x16x32_bf16 v[204:207], a[84:87], a[16:19], v[204:207]\n\t"
    "buffer_load_dwordx4 v12, s[48:51], s76 offen offset:0 lds\n\t"
    "v_mfma_f32_16x16x32_bf16 v[232:235], a[80:83], a[24:27], v[232:235]\n\t"
    "v_mfma_f32_16x16x32_bf16 v[248:251], a[80:83], a[28:31], v[248:251]\n\t"
    "v_mfma_f32_16x16x32_bf16 v[252:255], a[84:87], a[28:31], v[252:255]\n\t"
    "v_mfma_f32_16x16x32_bf16 v[236:239], a[84:87], a[24:27], v[236:239]\n\t"
    "v_mfma_f32_16x16x32_bf16 v[224:227], a[72:75], a[24:27], v[224:227]\n\t"
    "v_mfma_f32_16x16x32_bf16 v[240:243], a[72:75], a[28:31], v[240:243]\n\t"
    "buffer_load_dwordx4 v13, s[48:51], s76 offen offset:1024 lds\n\t"
    "v_mfma_f32_16x16x32_bf16 v[244:247], a[76:79], a[28:31], v[244:247]\n\t"
    "v_mfma_f32_16x16x32_bf16 v[228:231], a[76:79], a[24:27], v[228:231]\n\t"
    "s_waitcnt vmcnt(18) lgkmcnt(0)\n\t"
    "s_barrier\n\t"
    "v_mfma_f32_16x16x32_bf16 v[128:131], a[88:91], a[36:39], v[128:131]\n\t"
    "ds_read_b64_tr_b16 a[72:73], v24 offset:0\n\t"
    "v_mfma_f32_16x16x32_bf16 v[144:147], a[88:91], a[40:43], v[144:147]\n\t"
    "ds_read_b128 a[0:3], v18 offset:0\n\t"
    "v_mfma_f32_16x16x32_bf16 v[148:151], a[92:95], a[40:43], v[148:151]\n\t"
    "ds_read_b64_tr_b16 a[74:75], v24 offset:1024\n\t"
    "v_mfma_f32_16x16x32_bf16 v[132:135], a[92:95], a[36:39], v[132:135]\n\t"
    "buffer_load_dwordx4 v14, s[48:51], s76 offen offset:2048 lds\n\t"
    "v_mfma_f32_16x16x32_bf16 v[136:139], a[96:99], a[36:39], v[136:139]\n\t"
    "ds_read_b128 a[4:7], v18 offset:2048\n\t"
    "v_mfma_f32_16x16x32_bf16 v[152:155], a[96:99], a[40:43], v[152:155]\n\t"
    "ds_read_b64_tr_b16 a[76:77], v25 offset:0\n\t"
    "v_mfma_f32_16x16x32_bf16 v[156:159], a[100:103], a[40:43], v[156:159]\n\t"
    "ds_read_b128 a[8:11], v18 offset:4096\n\t"
    "v_mfma_f32_16x16x32_bf16 v[140:143], a[100:103], a[36:39], v[140:143]\n\t"
    "ds_read_b64_tr_b16 a[78:79], v25 offset:1024\n\t"
    "v_mfma_f32_16x16x32_bf16 v[168:171], a[96:99], a[44:47], v[168:171]\n\t"
    "ds_read_b128 a[12:15], v18 offset:6144\n\t"
    "v_mfma_f32_16x16x32_bf16 v[184:187], a[96:99], a[48:51], v[184:187]\n\t"
    "ds_read_b64_tr_b16 a[80:81], v26 offset:0\n\t"
    "v_mfma_f32_16x16x32_bf16 v[188:191], a[100:103], a[48:51], v[188:191]\n\t"
    "buffer_load_dwordx4 v15, s[48:51], s76 offen offset:3072 lds\n\t"
    "v_mfma_f32_16x16x32_bf16 v[172:175], a[100:103], a[44:47], v[172:175]\n\t"
    "ds_read_b128 a[16:19], v18 offset:8192\n\t"
    "v_mfma_f32_16x16x32_bf16 v[160:163], a[88:91], a[44:47], v[160:163]\n\t"
    "ds_read_b64_tr_b16 a[82:83], v26 offset:1024\n\t"
    "v_mfma_f32_16x16x32_bf16 v[176:179], a[88:91], a[48:51], v[176:179]\n\t"
    "ds_read_b128 a[20:23], v18 offset:10240\n\t"
    "v_mfma_f32_16x16x32_bf16 v[180:183], a[92:95], a[48:51], v[180:183]\n\t"
    "ds_read_b64_tr_b16 a[84:85], v27 offset:0\n\t"
    "v_mfma_f32_16x16x32_bf16 v[164:167], a[92:95], a[44:47], v[164:167]\n\t"
    "s_add_u32 m0, s36, 32768\n\t"
    "ds_read_b128 a[24:27], v18 offset:12288\n\t"
    "v_mfma_f32_16x16x32_bf16 v[192:195], a[88:91], a[52:55], v[192:195]\n\t"
    "buffer_load_dwordx4 v64, s[40:43], s69 offen offset:0 lds\n\t"
    "v_mfma_f32_16x16x32_bf16 v[208:211], a[88:91], a[56:59], v[208:211]\n\t"
    "ds_read_b64_tr_b16 a[86:87], v27 offset:1024\n\t"
    "v_mfma_f32_16x16x32_bf16 v[212:215], a[92:95], a[56:59], v[212:215]\n\t"
    "ds_read_b128 a[28:31], v18 offset:14336\n\t"
    "v_mfma_f32_16x16x32_bf16 v[196:199], a[92:95], a[52:55], v[196:199]\n\t"
    "v_mfma_f32_16x16x32_bf16 v[200:203], a[96:99], a[52:55], v[200:203]\n\t"
    "v_mfma_f32_16x16x32_bf16 v[216:219], a[96:99], a[56:59], v[216:219]\n\t"
    "v_mfma_f32_16x16x32_bf16 v[220:223], a[100:103], a[56:59], v[220:223]\n\t"
    "v_mfma_f32_16x16x32_bf16 v[204:207], a[100:103], a[52:55], v[204:207]\n\t"
    "buffer_load_dwordx4 v65, s[40:43], s69 offen offset:1024 lds\n\t"
    "v_mfma_f32_16x16x32_bf16 v[232:235], a[96:99], a[60:63], v[232:235]\n\t"
    "v_mfma_f32_16x16x32_bf16 v[248:251], a[96:99], a[64:67], v[248:251]\n\t"
    "v_mfma_f32_16x16x32_bf16 v[252:255], a[100:103], a[64:67], v[252:255]\n\t"
    "v_mfma_f32_16x16x32_bf16 v[236:239], a[100:103], a[60:63], v[236:239]\n\t"
    "v_mfma_f32_16x16x32_bf16 v[224:227], a[88:91], a[60:63], v[224:227]\n\t"
    "s_add_u32 m0, s37, 32768\n\t"
    "v_mfma_f32_16x16x32_bf16 v[240:243], a[88:91], a[64:67], v[240:243]\n\t"
    "buffer_load_dwordx4 v66, s[40:43], s69 offen offset:2048 lds\n\t"
    "v_mfma_f32_16x16x32_bf16 v[244:247], a[92:95], a[64:67], v[244:247]\n\t"
    "v_mfma_f32_16x16x32_bf16 v[228:231], a[92:95], a[60:63], v[228:231]\n\t"
    "s_waitcnt lgkmcnt(0)\n\t"
    "v_mfma_f32_16x16x32_bf16 v[128:131], a[72:75], a[0:3], v[128:131]\n\t"
    "ds_read_b64_tr_b16 a[88:89], v24 offset:8192\n\t"
    "v_mfma_f32_16x16x32_bf16 v[144:147], a[72:75], a[4:7], v[144:147]\n\t"
    "v_mfma_f32_16x16x32_bf16 v[148:151], a[76:79], a[4:7], v[148:151]\n\t"
    "ds_read_b128 a[32:35], v22 offset:0\n\t"
    "v_mfma_f32_16x16x32_bf16 v[132:135], a[76:79], a[0:3], v[132:135]\n\t"
    "buffer_load_dwordx4 v67, s[40:43], s69 offen offset:3072 lds\n\t"
    "v_mfma_f32_16x16x32_bf16 v[136:139], a[80:83], a[0:3], v[136:139]\n\t"
    "ds_read_b64_tr_b16 a[90:91], v24 offset:9216\n\t"
    "v_mfma_f32_16x16x32_bf16 v[152:155], a[80:83], a[4:7], v[152:155]\n\t"
    "v_mfma_f32_16x16x32_bf16 v[156:159], a[84:87], a[4:7], v[156:159]\n\t"
    "ds_read_b64_tr_b16 a[92:93], v25 offset:8192\n\t"
    "v_mfma_f32_16x16x32_bf16 v[140:143], a[84:87], a[0:3], v[140:143]\n\t"
    "v_mfma_f32_16x16x32_bf16 v[168:171], a[80:83], a[8:11], v[168:171]\n\t"
    "ds_read_b64_tr_b16 a[94:95], v25 offset:9216\n\t"
    "v_mfma_f32_16x16x32_bf16 v[184:187], a[80:83], a[12:15], v[184:187]\n\t"
    "s_add_u32 m0, s38, 32768\n\t"
    "v_mfma_f32_16x16x32_bf16 v[188:191], a[84:87], a[12:15], v[188:191]\n\t"
    "buffer_load_dwordx4 v68, s[40:43], s69 offen offset:0 lds\n\t"
    "v_mfma_f32_16x16x32_bf16 v[172:175], a[84:87], a[8:11], v[172:175]\n\t"
    "ds_read_b64_tr_b16 a[96:97], v26 offset:8192\n\t"
    "v_mfma_f32_16x16x32_bf16 v[160:163], a[72:75], a[8:11], v[160:163]\n\t"
    "v_mfma_f32_16x16x32_bf16 v[176:179], a[72:75], a[12:15], v[176:179]\n\t"
    "ds_read_b64_tr_b16 a[98:99], v26 offset:9216\n\t"
    "v_mfma_f32_16x16x32_bf16 v[180:183], a[76:79], a[12:15], v[180:183]\n\t"
    "v_mfma_f32_16x16x32_bf16 v[164:167], a[76:79], a[8:11], v[164:167]\n\t"
    "ds_read_b64_tr_b16 a[100:101], v27 offset:8192\n\t"
    "v_mfma_f32_16x16x32_bf16 v[192:195], a[72:75], a[16:19], v[192:195]\n\t"
    "buffer_load_dwordx4 v69, s[40:43], s69 offen offset:1024 lds\n\t"
    "v_mfma_f32_16x16x32_bf16 v[208:211], a[72:75], a[20:23], v[208:211]\n\t"
    "ds_read_b64_tr_b16 a[102:103], v27 offset:9216\n\t"
    "v_mfma_f32_16x16x32_bf16 v[212:215], a[76:79], a[20:23], v[212:215]\n\t"
    "v_mfma_f32_16x16x32_bf16 v[196:199], a[76:79], a[16:19], v[196:199]\n\t"
    "v_mfma_f32_16x16x32_bf16 v[200:203], a[80:83], a[16:19], v[200:203]\n\t"
    "v_mfma_f32_16x16x32_bf16 v[216:219], a[80:83], a[20:23], v[216:219]\n\t"
    "v_mfma_f32_16x16x32_bf16 v[220:223], a[84:87], a[20:23], v[220:223]\n\t"
    "s_add_u32 m0, s39, 32768\n\t"
    "v_mfma_f32_16x16x32_bf16 v[204:207], a[84:87], a[16:19], v[204:207]\n\t"
    "buffer_load_dwordx4 v70, s[40:43], s69 offen offset:2048 lds\n\t"
    "v_mfma_f32_16x16x32_bf16 v[232:235], a[80:83], a[24:27], v[232:235]\n\t"
    "v_mfma_f32_16x16x32_bf16 v[248:251], a[80:83], a[28:31], v[248:251]\n\t"
    "v_mfma_f32_16x16x32_bf16 v[252:255], a[84:87], a[28:31], v[252:255]\n\t"
    "v_mfma_f32_16x16x32_bf16 v[236:239], a[84:87], a[24:27], v[236:239]\n\t"
    "v_mfma_f32_16x16x32_bf16 v[224:227], a[72:75], a[24:27], v[224:227]\n\t"
    "v_mfma_f32_16x16x32_bf16 v[240:243], a[72:75], a[28:31], v[240:243]\n\t"
    "buffer_load_dwordx4 v71, s[40:43], s69 offen offset:3072 lds\n\t"
    "v_mfma_f32_16x16x32_bf16 v[244:247], a[76:79], a[28:31], v[244:247]\n\t"
    "v_mfma_f32_16x16x32_bf16 v[228:231], a[76:79], a[24:27], v[228:231]\n\t"
    "s_waitcnt vmcnt(24) lgkmcnt(0)\n\t"
    "s_barrier\n\t"
    "v_mfma_f32_16x16x32_bf16 v[128:131], a[88:91], a[4:7], v[128:131]\n\t"
    "ds_read_b64_tr_b16 a[72:73], v24 offset:16384\n\t"
    "v_mfma_f32_16x16x32_bf16 v[144:147], a[88:91], a[8:11], v[144:147]\n\t"
    "ds_read_b128 a[36:39], v19 offset:0\n\t"
    "v_mfma_f32_16x16x32_bf16 v[148:151], a[92:95], a[8:11], v[148:151]\n\t"
    "s_add_u32 m0, s27, 49152\n\t"
    "ds_read_b64_tr_b16 a[74:75], v24 offset:17408\n\t"
    "v_mfma_f32_16x16x32_bf16 v[132:135], a[92:95], a[4:7], v[132:135]\n\t"
    "buffer_load_dwordx4 v8, s[48:51], s77 offen offset:0 lds\n\t"
    "v_mfma_f32_16x16x32_bf16 v[136:139], a[96:99], a[4:7], v[136:139]\n\t"
    "ds_read_b128 a[40:43], v19 offset:2048\n\t"
    "v_mfma_f32_16x16x32_bf16 v[152:155], a[96:99], a[8:11], v[152:155]\n\t"
    "ds_read_b64_tr_b16 a[76:77], v25 offset:16384\n\t"
    "v_mfma_f32_16x16x32_bf16 v[156:159], a[100:103], a[8:11], v[156:159]\n\t"
    "ds_read_b128 a[44:47], v19 offset:4096\n\t"
    "v_mfma_f32_16x16x32_bf16 v[140:143], a[100:103], a[4:7], v[140:143]\n\t"
    "ds_read_b64_tr_b16 a[78:79], v25 offset:17408\n\t"
    "v_mfma_f32_16x16x32_bf16 v[168:171], a[96:99], a[12:15], v[168:171]\n\t"
    "ds_read_b128 a[48:51], v19 offset:6144\n\t"
    "v_mfma_f32_16x16x32_bf16 v[184:187], a[96:99], a[16:19], v[184:187]\n\t"
    "ds_read_b64_tr_b16 a[80:81], v26 offset:16384\n\t"
    "v_mfma_f32_16x16x32_bf16 v[188:191], a[100:103], a[16:19], v[188:191]\n\t"
    "buffer_load_dwordx4 v9, s[48:51], s77 offen offset:1024 lds\n\t"
    "v_mfma_f32_16x16x32_bf16 v[172:175], a[100:103], a[12:15], v[172:175]\n\t"
    "ds_read_b128 a[52:55], v19 offset:8192\n\t"
    "v_mfma_f32_16x16x32_bf16 v[160:163], a[88:91], a[12:15], v[160:163]\n\t"
    "ds_read_b64_tr_b16 a[82:83], v26 offset:17408\n\t"
    "v_mfma_f32_16x16x32_bf16 v[176:179], a[88:91], a[16:19], v[176:179]\n\t"
    "ds_read_b128 a[56:59], v19 offset:10240\n\t"
    "v_mfma_f32_16x16x32_bf16 v[180:183], a[92:95], a[16:19], v[180:183]\n\t"
    "ds_read_b64_tr_b16 a[84:85], v27 offset:16384\n\t"
    "v_mfma_f32_16x16x32_bf16 v[164:167], a[92:95], a[12:15], v[164:167]\n\t"
    "ds_read_b128 a[60:63], v19 offset:12288\n\t"
    "v_mfma_f32_16x16x32_bf16 v[192:195], a[88:91], a[20:23], v[192:195]\n\t"
    "buffer_load_dwordx4 v10, s[48:51], s77 offen offset:2048 lds\n\t"
    "v_mfma_f32_16x16x32_bf16 v[208:211], a[88:91], a[24:27], v[208:211]\n\t"
    "ds_read_b64_tr_b16 a[86:87], v27 offset:17408\n\t"
    "v_mfma_f32_16x16x32_bf16 v[212:215], a[92:95], a[24:27], v[212:215]\n\t"
    "ds_read_b128 a[64:67], v19 offset:14336\n\t"
    "v_mfma_f32_16x16x32_bf16 v[196:199], a[92:95], a[20:23], v[196:199]\n\t"
    "v_mfma_f32_16x16x32_bf16 v[200:203], a[96:99], a[20:23], v[200:203]\n\t"
    "v_mfma_f32_16x16x32_bf16 v[216:219], a[96:99], a[24:27], v[216:219]\n\t"
    "v_mfma_f32_16x16x32_bf16 v[220:223], a[100:103], a[24:27], v[220:223]\n\t"
    "v_mfma_f32_16x16x32_bf16 v[204:207], a[100:103], a[20:23], v[204:207]\n\t"
    "buffer_load_dwordx4 v11, s[48:51], s77 offen offset:3072 lds\n\t"
    "v_mfma_f32_16x16x32_bf16 v[232:235], a[96:99], a[28:31], v[232:235]\n\t"
    "v_mfma_f32_16x16x32_bf16 v[248:251], a[96:99], a[32:35], v[248:251]\n\t"
    "v_mfma_f32_16x16x32_bf16 v[252:255], a[100:103], a[32:35], v[252:255]\n\t"
    "v_mfma_f32_16x16x32_bf16 v[236:239], a[100:103], a[28:31], v[236:239]\n\t"
    "v_mfma_f32_16x16x32_bf16 v[224:227], a[88:91], a[28:31], v[224:227]\n\t"
    "s_add_u32 m0, s27, 0\n\t"
    "v_mfma_f32_16x16x32_bf16 v[240:243], a[88:91], a[32:35], v[240:243]\n\t"
    "buffer_load_dwordx4 v8, s[52:55], s77 offen offset:0 lds\n\t"
    "v_mfma_f32_16x16x32_bf16 v[244:247], a[92:95], a[32:35], v[244:247]\n\t"
    "v_mfma_f32_16x16x32_bf16 v[228:231], a[92:95], a[28:31], v[228:231]\n\t"
    "s_waitcnt lgkmcnt(0)\n\t"
    "v_mfma_f32_16x16x32_bf16 v[128:131], a[72:75], a[36:39], v[128:131]\n\t"
    "ds_read_b64_tr_b16 a[88:89], v24 offset:24576\n\t"
    "v_mfma_f32_16x16x32_bf16 v[144:147], a[72:75], a[40:43], v[144:147]\n\t"
    "v_mfma_f32_16x16x32_bf16 v[148:151], a[76:79], a[40:43], v[148:151]\n\t"
    "ds_read_b128 a[68:71], v23 offset:0\n\t"
    "v_mfma_f32_16x16x32_bf16 v[132:135], a[76:79], a[36:39], v[132:135]\n\t"
    "v_mfma_f32_16x16x32_bf16 v[136:139], a[80:83], a[36:39], v[136:139]\n\t"
    "buffer_load_dwordx4 v9, s[52:55], s77 offen offset:1024 lds\n\t"
    "v_mfma_f32_16x16x32_bf16 v[152:155], a[80:83], a[40:43], v[152:155]\n\t"
    "ds_read_b64_tr_b16 a[90:91], v24 offset:25600\n\t"
    "v_mfma_f32_16x16x32_bf16 v[156:159], a[84:87], a[40:43], v[156:159]\n\t"
    "v_mfma_f32_16x16x32_bf16 v[140:143], a[84:87], a[36:39], v[140:143]\n\t"
    "ds_read_b64_tr_b16 a[92:93], v25 offset:24576\n\t"
    "v_mfma_f32_16x16x32_bf16 v[168:171], a[80:83], a[44:47], v[168:171]\n\t"
    "v_mfma_f32_16x16x32_bf16 v[184:187], a[80:83], a[48:51], v[184:187]\n\t"
    "ds_read_b64_tr_b16 a[94:95], v25 offset:25600\n\t"
    "v_mfma_f32_16x16x32_bf16 v[188:191], a[84:87], a[48:51], v[188:191]\n\t"
    "v_mfma_f32_16x16x32_bf16 v[172:175], a[84:87], a[44:47], v[172:175]\n\t"
    "ds_read_b64_tr_b16 a[96:97], v26 offset:24576\n\t"
    "v_mfma_f32_16x16x32_bf16 v[160:163], a[72:75], a[44:47], v[160:163]\n\t"
    "buffer_load_dwordx4 v10, s[52:55], s77 offen offset:2048 lds\n\t"
    "v_mfma_f32_16x16x32_bf16 v[176:179], a[72:75], a[48:51], v[176:179]\n\t"
    "ds_read_b64_tr_b16 a[98:99], v26 offset:25600\n\t"
    "v_mfma_f32_16x16x32_bf16 v[180:183], a[76:79], a[48:51], v[180:183]\n\t"
    "v_mfma_f32_16x16x32_bf16 v[164:167], a[76:79], a[44:47], v[164:167]\n\t"
    "ds_read_b64_tr_b16 a[100:101], v27 offset:24576\n\t"
    "v_mfma_f32_16x16x32_bf16 v[192:195], a[72:75], a[52:55], v[192:195]\n\t"
    "v_mfma_f32_16x16x32_bf16 v[208:211], a[72:75], a[56:59], v[208:211]\n\t"
    "ds_read_b64_tr_b16 a[102:103], v27 offset:25600\n\t"
    "v_mfma_f32_16x16x32_bf16 v[212:215], a[76:79], a[56:59], v[212:215]\n\t"
    "v_mfma_f32_16x16x32_bf16 v[196:199], a[76:79], a[52:55], v[196:199]\n\t"
    "v_mfma_f32_16x16x32_bf16 v[200:203], a[80:83], a[52:55], v[200:203]\n\t"
    "buffer_load_dwordx4 v11, s[52:55], s77 offen offset:3072 lds\n\t"
    "v_mfma_f32_16x16x32_bf16 v[216:219], a[80:83], a[56:59], v[216:219]\n\t"
    "v_mfma_f32_16x16x32_bf16 v[220:223], a[84:87], a[56:59], v[220:223]\n\t"
    "v_mfma_f32_16x16x32_bf16 v[204:207], a[84:87], a[52:55], v[204:207]\n\t"
    "v_mfma_f32_16x16x32_bf16 v[232:235], a[80:83], a[60:63], v[232:235]\n\t"
    "v_mfma_f32_16x16x32_bf16 v[248:251], a[80:83], a[64:67], v[248:251]\n\t"
    "v_mfma_f32_16x16x32_bf16 v[252:255], a[84:87], a[64:67], v[252:255]\n\t"
    "v_mfma_f32_16x16x32_bf16 v[236:239], a[84:87], a[60:63], v[236:239]\n\t"
    "v_mfma_f32_16x16x32_bf16 v[224:227], a[72:75], a[60:63], v[224:227]\n\t"
    "buffer_load_dword v59, v62, s[84:87], 0 offen\n\t"
    "v_mfma_f32_16x16x32_bf16 v[240:243], a[72:75], a[64:67], v[240:243]\n\t"
    "v_mfma_f32_16x16x32_bf16 v[244:247], a[76:79], a[64:67], v[244:247]\n\t"
    "v_mfma_f32_16x16x32_bf16 v[228:231], a[76:79], a[60:63], v[228:231]\n\t"
    "s_waitcnt vmcnt(17) lgkmcnt(0)\n\t"
    "s_barrier\n\t"
    "v_mfma_f32_16x16x32_bf16 v[128:131], a[88:91], a[40:43], v[128:131]\n\t"
    "ds_read_b64_tr_b16 a[72:73], v24 offset:32768\n\t"
    "v_mfma_f32_16x16x32_bf16 v[144:147], a[88:91], a[44:47], v[144:147]\n\t"
    "ds_read_b128 a[0:3], v16 offset:0\n\t"
    "v_mfma_f32_16x16x32_bf16 v[148:151], a[92:95], a[44:47], v[148:151]\n\t"
    "s_add_u32 m0, s36, 65536\n\t"
    "ds_read_b64_tr_b16 a[74:75], v24 offset:33792\n\t"
    "v_mfma_f32_16x16x32_bf16 v[132:135], a[92:95], a[40:43], v[132:135]\n\t"
    "buffer_load_dwordx4 v64, s[44:47], s69 offen offset:0 lds\n\t"
    "v_mfma_f32_16x16x32_bf16 v[136:139], a[96:99], a[40:43], v[136:139]\n\t"
    "ds_read_b128 a[4:7], v16 offset:2048\n\t"
    "v_mfma_f32_16x16x32_bf16 v[152:155], a[96:99], a[44:47], v[152:155]\n\t"
    "ds_read_b64_tr_b16 a[76:77], v25 offset:32768\n\t"
    "v_mfma_f32_16x16x32_bf16 v[156:159], a[100:103], a[44:47], v[156:159]\n\t"
    "ds_read_b128 a[8:11], v16 offset:4096\n\t"
    "v_mfma_f32_16x16x32_bf16 v[140:143], a[100:103], a[40:43], v[140:143]\n\t"
    "ds_read_b64_tr_b16 a[78:79], v25 offset:33792\n\t"
    "v_mfma_f32_16x16x32_bf16 v[168:171], a[96:99], a[48:51], v[168:171]\n\t"
    "ds_read_b128 a[12:15], v16 offset:6144\n\t"
    "v_mfma_f32_16x16x32_bf16 v[184:187], a[96:99], a[52:55], v[184:187]\n\t"
    "ds_read_b64_tr_b16 a[80:81], v26 offset:32768\n\t"
    "v_mfma_f32_16x16x32_bf16 v[188:191], a[100:103], a[52:55], v[188:191]\n\t"
    "buffer_load_dwordx4 v65, s[44:47], s69 offen offset:1024 lds\n\t"
    "v_mfma_f32_16x16x32_bf16 v[172:175], a[100:103], a[48:51], v[172:175]\n\t"
    "ds_read_b128 a[16:19], v16 offset:8192\n\t"
    "v_mfma_f32_16x16x32_bf16 v[160:163], a[88:91], a[48:51], v[160:163]\n\t"
    "ds_read_b64_tr_b16 a[82:83], v26 offset:33792\n\t"
    "v_mfma_f32_16x16x32_bf16 v[176:179], a[88:91], a[52:55], v[176:179]\n\t"
    "ds_read_b128 a[20:23], v16 offset:10240\n\t"
    "v_mfma_f32_16x16x32_bf16 v[180:183], a[92:95], a[52:55], v[180:183]\n\t"
    "ds_read_b64_tr_b16 a[84:85], v27 offset:32768\n\t"
    "v_mfma_f32_16x16x32_bf16 v[164:167], a[92:95], a[48:51], v[164:167]\n\t"
    "s_add_u32 m0, s37, 65536\n\t"
    "ds_read_b128 a[24:27], v16 offset:12288\n\t"
    "v_mfma_f32_16x16x32_bf16 v[192:195], a[88:91], a[56:59], v[192:195]\n\t"
    "buffer_load_dwordx4 v66, s[44:47], s69 offen offset:2048 lds\n\t"
    "v_mfma_f32_16x16x32_bf16 v[208:211], a[88:91], a[60:63], v[208:211]\n\t"
    "ds_read_b64_tr_b16 a[86:87], v27 offset:33792\n\t"
    "v_mfma_f32_16x16x32_bf16 v[212:215], a[92:95], a[60:63], v[212:215]\n\t"
    "ds_read_b128 a[28:31], v16 offset:14336\n\t"
    "v_mfma_f32_16x16x32_bf16 v[196:199], a[92:95], a[56:59], v[196:199]\n\t"
    "v_mfma_f32_16x16x32_bf16 v[200:203], a[96:99], a[56:59], v[200:203]\n\t"
    "v_mfma_f32_16x16x32_bf16 v[216:219], a[96:99], a[60:63], v[216:219]\n\t"
    "v_mfma_f32_16x16x32_bf16 v[220:223], a[100:103], a[60:63], v[220:223]\n\t"
    "v_mfma_f32_16x16x32_bf16 v[204:207], a[100:103], a[56:59], v[204:207]\n\t"
    "buffer_load_dwordx4 v67, s[44:47], s69 offen offset:3072 lds\n\t"
    "v_mfma_f32_16x16x32_bf16 v[232:235], a[96:99], a[64:67], v[232:235]\n\t"
    "v_mfma_f32_16x16x32_bf16 v[248:251], a[96:99], a[68:71], v[248:251]\n\t"
    "v_mfma_f32_16x16x32_bf16 v[252:255], a[100:103], a[68:71], v[252:255]\n\t"
    "v_mfma_f32_16x16x32_bf16 v[236:239], a[100:103], a[64:67], v[236:239]\n\t"
    "v_mfma_f32_16x16x32_bf16 v[224:227], a[88:91], a[64:67], v[224:227]\n\t"
    "s_add_u32 m0, s38, 65536\n\t"
    "v_mfma_f32_16x16x32_bf16 v[240:243], a[88:91], a[68:71], v[240:243]\n\t"
    "buffer_load_dwordx4 v68, s[44:47], s69 offen offset:0 lds\n\t"
    "v_mfma_f32_16x16x32_bf16 v[244:247], a[92:95], a[68:71], v[244:247]\n\t"
    "v_mfma_f32_16x16x32_bf16 v[228:231], a[92:95], a[64:67], v[228:231]\n\t"
    "s_waitcnt lgkmcnt(0)\n\t"
    "v_mfma_f32_16x16x32_bf16 v[128:131], a[72:75], a[0:3], v[128:131]\n\t"
    "ds_read_b64_tr_b16 a[88:89], v24 offset:40960\n\t"
    "v_mfma_f32_16x16x32_bf16 v[144:147], a[72:75], a[4:7], v[144:147]\n\t"
    "ds_read_b128 a[36:39], v17 offset:0\n\t"
    "v_mfma_f32_16x16x32_bf16 v[148:151], a[76:79], a[4:7], v[148:151]\n\t"
    "ds_read_b64_tr_b16 a[90:91], v24 offset:41984\n\t"
    "v_mfma_f32_16x16x32_bf16 v[132:135], a[76:79], a[0:3], v[132:135]\n\t"
    "buffer_load_dwordx4 v69, s[44:47], s69 offen offset:1024 lds\n\t"
    "v_mfma_f32_16x16x32_bf16 v[136:139], a[80:83], a[0:3], v[136:139]\n\t"
    "ds_read_b128 a[40:43], v17 offset:2048\n\t"
    "v_mfma_f32_16x16x32_bf16 v[152:155], a[80:83], a[4:7], v[152:155]\n\t"
    "ds_read_b64_tr_b16 a[92:93], v25 offset:40960\n\t"
    "v_mfma_f32_16x16x32_bf16 v[156:159], a[84:87], a[4:7], v[156:159]\n\t"
    "ds_read_b128 a[44:47], v17 offset:4096\n\t"
    "v_mfma_f32_16x16x32_bf16 v[140:143], a[84:87], a[0:3], v[140:143]\n\t"
    "ds_read_b64_tr_b16 a[94:95], v25 offset:41984\n\t"
    "v_mfma_f32_16x16x32_bf16 v[168:171], a[80:83], a[8:11], v[168:171]\n\t"
    "ds_read_b128 a[48:51], v17 offset:6144\n\t"
    "v_mfma_f32_16x16x32_bf16 v[184:187], a[80:83], a[12:15], v[184:187]\n\t"
    "s_add_u32 m0, s39, 65536\n\t"
    "ds_read_b64_tr_b16 a[96:97], v26 offset:40960\n\t"
    "v_mfma_f32_16x16x32_bf16 v[188:191], a[84:87], a[12:15], v[188:191]\n\t"
    "buffer_load_dwordx4 v70, s[44:47], s69 offen offset:2048 lds\n\t"
    "v_mfma_f32_16x16x32_bf16 v[172:175], a[84:87], a[8:11], v[172:175]\n\t"
    "ds_read_b128 a[52:55], v17 offset:8192\n\t"
    "v_mfma_f32_16x16x32_bf16 v[160:163], a[72:75], a[8:11], v[160:163]\n\t"
    "ds_read_b64_tr_b16 a[98:99], v26 offset:41984\n\t"
    "v_mfma_f32_16x16x32_bf16 v[176:179], a[72:75], a[12:15], v[176:179]\n\t"
    "ds_read_b128 a[56:59], v17 offset:10240\n\t"
    "v_mfma_f32_16x16x32_bf16 v[180:183], a[76:79], a[12:15], v[180:183]\n\t"
    "ds_read_b64_tr_b16 a[100:101], v27 offset:40960\n\t"
    "v_mfma_f32_16x16x32_bf16 v[164:167], a[76:79], a[8:11], v[164:167]\n\t"
    "ds_read_b128 a[60:63], v17 offset:12288\n\t"
    "v_mfma_f32_16x16x32_bf16 v[192:195], a[72:75], a[16:19], v[192:195]\n\t"
    "buffer_load_dwordx4 v71, s[44:47], s69 offen offset:3072 lds\n\t"
    "v_mfma_f32_16x16x32_bf16 v[208:211], a[72:75], a[20:23], v[208:211]\n\t"
    "ds_read_b64_tr_b16 a[102:103], v27 offset:41984\n\t"
    "v_mfma_f32_16x16x32_bf16 v[212:215], a[76:79], a[20:23], v[212:215]\n\t"
    "ds_read_b128 a[64:67], v17 offset:14336\n\t"
    "v_mfma_f32_16x16x32_bf16 v[196:199], a[76:79], a[16:19], v[196:199]\n\t"
    "v_mfma_f32_16x16x32_bf16 v[200:203], a[80:83], a[16:19], v[200:203]\n\t"
    "v_mfma_f32_16x16x32_bf16 v[216:219], a[80:83], a[20:23], v[216:219]\n\t"
    "v_mfma_f32_16x16x32_bf16 v[220:223], a[84:87], a[20:23], v[220:223]\n\t"
    "s_add_u32 m0, s27, 16384\n\t"
    "v_mfma_f32_16x16x32_bf16 v[204:207], a[84:87], a[16:19], v[204:207]\n\t"
    "buffer_load_dwordx4 v12, s[48:51], s77 offen offset:0 lds\n\t"
    "v_mfma_f32_16x16x32_bf16 v[232:235], a[80:83], a[24:27], v[232:235]\n\t"
    "v_mfma_f32_16x16x32_bf16 v[248:251], a[80:83], a[28:31], v[248:251]\n\t"
    "v_mfma_f32_16x16x32_bf16 v[252:255], a[84:87], a[28:31], v[252:255]\n\t"
    "v_mfma_f32_16x16x32_bf16 v[236:239], a[84:87], a[24:27], v[236:239]\n\t"
    "v_mfma_f32_16x16x32_bf16 v[224:227], a[72:75], a[24:27], v[224:227]\n\t"
    "v_mfma_f32_16x16x32_bf16 v[240:243], a[72:75], a[28:31], v[240:243]\n\t"
    "buffer_load_dwordx4 v13, s[48:51], s77 offen offset:1024 lds\n\t"
    "v_mfma_f32_16x16x32_bf16 v[244:247], a[76:79], a[28:31], v[244:247]\n\t"
    "v_mfma_f32_16x16x32_bf16 v[228:231], a[76:79], a[24:27], v[228:231]\n\t"
    "s_waitcnt vmcnt(15) lgkmcnt(0)\n\t"
    "s_barrier\n\t"
    "v_mfma_f32_16x16x32_bf16 v[128:131], a[88:91], a[36:39], v[128:131]\n\t"
    "ds_read_b64_tr_b16 a[72:73], v24 offset:49152\n\t"
    "v_mfma_f32_16x16x32_bf16 v[144:147], a[88:91], a[40:43], v[144:147]\n\t"
    "ds_read_b128 a[0:3], v16 offset:32768\n\t"
    "v_mfma_f32_16x16x32_bf16 v[148:151], a[92:95], a[40:43], v[148:151]\n\t"
    "ds_read_b64_tr_b16 a[74:75], v24 offset:50176\n\t"
    "v_mfma_f32_16x16x32_bf16 v[132:135], a[92:95], a[36:39], v[132:135]\n\t"
    "buffer_load_dwordx4 v14, s[48:51], s77 offen offset:2048 lds\n\t"
    "v_mfma_f32_16x16x32_bf16 v[136:139], a[96:99], a[36:39], v[136:139]\n\t"
    "ds_read_b128 a[4:7], v16 offset:34816\n\t"
    "v_mfma_f32_16x16x32_bf16 v[152:155], a[96:99], a[40:43], v[152:155]\n\t"
    "ds_read_b64_tr_b16 a[76:77], v25 offset:49152\n\t"
    "v_mfma_f32_16x16x32_bf16 v[156:159], a[100:103], a[40:43], v[156:159]\n\t"
    "ds_read_b128 a[8:11], v16 offset:36864\n\t"
    "v_mfma_f32_16x16x32_bf16 v[140:143], a[100:103], a[36:39], v[140:143]\n\t"
    "ds_read_b64_tr_b16 a[78:79], v25 offset:50176\n\t"
    "v_mfma_f32_16x16x32_bf16 v[168:171], a[96:99], a[44:47], v[168:171]\n\t"
    "buffer_load_dwordx4 v15, s[48:51], s77 offen offset:3072 lds\n\t"
    "v_mfma_f32_16x16x32_bf16 v[184:187], a[96:99], a[48:51], v[184:187]\n\t"
    "ds_read_b128 a[12:15], v16 offset:38912\n\t"
    "v_mfma_f32_16x16x32_bf16 v[188:191], a[100:103], a[48:51], v[188:191]\n\t"
    "ds_read_b64_tr_b16 a[80:81], v26 offset:49152\n\t"
    "v_mfma_f32_16x16x32_bf16 v[172:175], a[100:103], a[44:47], v[172:175]\n\t"
    "ds_read_b128 a[16:19], v16 offset:40960\n\t"
    "v_mfma_f32_16x16x32_bf16 v[160:163], a[88:91], a[44:47], v[160:163]\n\t"
    "s_add_u32 m0, s36, 0\n\t"
    "ds_read_b64_tr_b16 a[82:83], v26 offset:50176\n\t"
    "v_mfma_f32_16x16x32_bf16 v[176:179], a[88:91], a[48:51], v[176:179]\n\t"
    "buffer_load_dwordx4 v64, s[44:47], s70 offen offset:0 lds\n\t"
    "v_mfma_f32_16x16x32_bf16 v[180:183], a[92:95], a[48:51], v[180:183]\n\t"
    "ds_read_b128 a[20:23], v16 offset:43008\n\t"
    "v_mfma_f32_16x16x32_bf16 v[164:167], a[92:95], a[44:47], v[164:167]\n\t"
    "ds_read_b64_tr_b16 a[84:85], v27 offset:49152\n\t"
    "v_mfma_f32_16x16x32_bf16 v[192:195], a[88:91], a[52:55], v[192:195]\n\t"
    "ds_read_b128 a[24:27], v16 offset:45056\n\t"
    "v_mfma_f32_16x16x32_bf16 v[208:211], a[88:91], a[56:59], v[208:211]\n\t"
    "ds_read_b64_tr_b16 a[86:87], v27 offset:50176\n\t"
    "v_mfma_f32_16x16x32_bf16 v[212:215], a[92:95], a[56:59], v[212:215]\n\t"
    "ds_read_b128 a[28:31], v16 offset:47104\n\t"
    "v_mfma_f32_16x16x32_bf16 v[196:199], a[92:95], a[52:55], v[196:199]\n\t"
    "buffer_load_dwordx4 v65, s[44:47], s70 offen offset:1024 lds\n\t"
    "v_mfma_f32_16x16x32_bf16 v[200:203], a[96:99], a[52:55], v[200:203]\n\t"
    "v_mfma_f32_16x16x32_bf16 v[216:219], a[96:99], a[56:59], v[216:219]\n\t"
    "v_mfma_f32_16x16x32_bf16 v[220:223], a[100:103], a[56:59], v[220:223]\n\t"
    "v_mfma_f32_16x16x32_bf16 v[204:207], a[100:103], a[52:55], v[204:207]\n\t"
    "s_add_u32 m0, s37, 0\n\t"
    "v_mfma_f32_16x16x32_bf16 v[232:235], a[96:99], a[60:63], v[232:235]\n\t"
    "buffer_load_dwordx4 v66, s[44:47], s70 offen offset:2048 lds\n\t"
    "v_mfma_f32_16x16x32_bf16 v[248:251], a[96:99], a[64:67], v[248:251]\n\t"
    "v_mfma_f32_16x16x32_bf16 v[252:255], a[100:103], a[64:67], v[252:255]\n\t"
    "v_mfma_f32_16x16x32_bf16 v[236:239], a[100:103], a[60:63], v[236:239]\n\t"
    "v_mfma_f32_16x16x32_bf16 v[224:227], a[88:91], a[60:63], v[224:227]\n\t"
    "v_mfma_f32_16x16x32_bf16 v[240:243], a[88:91], a[64:67], v[240:243]\n\t"
    "buffer_load_dwordx4 v67, s[44:47], s70 offen offset:3072 lds\n\t"
    "v_mfma_f32_16x16x32_bf16 v[244:247], a[92:95], a[64:67], v[244:247]\n\t"
    "v_mfma_f32_16x16x32_bf16 v[228:231], a[92:95], a[60:63], v[228:231]\n\t"
    "s_waitcnt lgkmcnt(0)\n\t"
    "v_mfma_f32_16x16x32_bf16 v[128:131], a[72:75], a[0:3], v[128:131]\n\t"
    "ds_read_b64_tr_b16 a[88:89], v24 offset:57344\n\t"
    "v_mfma_f32_16x16x32_bf16 v[144:147], a[72:75], a[4:7], v[144:147]\n\t"
    "v_mfma_f32_16x16x32_bf16 v[148:151], a[76:79], a[4:7], v[148:151]\n\t"
    "s_add_u32 m0, s38, 0\n\t"
    "ds_read_b128 a[32:35], v20 offset:32768\n\t"
    "v_mfma_f32_16x16x32_bf16 v[132:135], a[76:79], a[0:3], v[132:135]\n\t"
    "buffer_load_dwordx4 v68, s[44:47], s70 offen offset:0 lds\n\t"
    "v_mfma_f32_16x16x32_bf16 v[136:139], a[80:83], a[0:3], v[136:139]\n\t"
    "ds_read_b64_tr_b16 a[90:91], v24 offset:58368\n\t"
    "v_mfma_f32_16x16x32_bf16 v[152:155], a[80:83], a[4:7], v[152:155]\n\t"
    "v_mfma_f32_16x16x32_bf16 v[156:159], a[84:87], a[4:7], v[156:159]\n\t"
    "ds_read_b64_tr_b16 a[92:93], v25 offset:57344\n\t"
    "v_mfma_f32_16x16x32_bf16 v[140:143], a[84:87], a[0:3], v[140:143]\n\t"
    "v_mfma_f32_16x16x32_bf16 v[168:171], a[80:83], a[8:11], v[168:171]\n\t"
    "ds_read_b64_tr_b16 a[94:95], v25 offset:58368\n\t"
    "v_mfma_f32_16x16x32_bf16 v[184:187], a[80:83], a[12:15], v[184:187]\n\t"
    "v_mfma_f32_16x16x32_bf16 v[188:191], a[84:87], a[12:15], v[188:191]\n\t"
    "buffer_load_dwordx4 v69, s[44:47], s70 offen offset:1024 lds\n\t"
    "v_mfma_f32_16x16x32_bf16 v[172:175], a[84:87], a[8:11], v[172:175]\n\t"
    "ds_read_b64_tr_b16 a[96:97], v26 offset:57344\n\t"
    "v_mfma_f32_16x16x32_bf16 v[160:163], a[72:75], a[8:11], v[160:163]\n\t"
    "v_mfma_f32_16x16x32_bf16 v[176:179], a[72:75], a[12:15], v[176:179]\n\t"
    "ds_read_b64_tr_b16 a[98:99], v26 offset:58368\n\t"
    "v_mfma_f32_16x16x32_bf16 v[180:183], a[76:79], a[12:15], v[180:183]\n\t"
    "v_mfma_f32_16x16x32_bf16 v[164:167], a[76:79], a[8:11], v[164:167]\n\t"
    "s_add_u32 m0, s39, 0\n\t"
    "ds_read_b64_tr_b16 a[100:101], v27 offset:57344\n\t"
    "v_mfma_f32_16x16x32_bf16 v[192:195], a[72:75], a[16:19], v[192:195]\n\t"
    "buffer_load_dwordx4 v70, s[44:47], s70 offen offset:2048 lds\n\t"
    "v_mfma_f32_16x16x32_bf16 v[208:211], a[72:75], a[20:23], v[208:211]\n\t"
    "ds_read_b64_tr_b16 a[102:103], v27 offset:58368\n\t"
    "v_mfma_f32_16x16x32_bf16 v[212:215], a[76:79], a[20:23], v[212:215]\n\t"
    "v_mfma_f32_16x16x32_bf16 v[196:199], a[76:79], a[16:19], v[196:199]\n\t"
    "v_mfma_f32_16x16x32_bf16 v[200:203], a[80:83], a[16:19], v[200:203]\n\t"
    "v_mfma_f32_16x16x32_bf16 v[216:219], a[80:83], a[20:23], v[216:219]\n\t"
    "v_mfma_f32_16x16x32_bf16 v[220:223], a[84:87], a[20:23], v[220:223]\n\t"
    "v_mfma_f32_16x16x32_bf16 v[204:207], a[84:87], a[16:19], v[204:207]\n\t"
    "buffer_load_dwordx4 v71, s[44:47], s70 offen offset:3072 lds\n\t"
    "v_mfma_f32_16x16x32_bf16 v[232:235], a[80:83], a[24:27], v[232:235]\n\t"
    "v_mfma_f32_16x16x32_bf16 v[248:251], a[80:83], a[28:31], v[248:251]\n\t"
    "v_mfma_f32_16x16x32_bf16 v[252:255], a[84:87], a[28:31], v[252:255]\n\t"
    "v_mfma_f32_16x16x32_bf16 v[236:239], a[84:87], a[24:27], v[236:239]\n\t"
    "v_mfma_f32_16x16x32_bf16 v[224:227], a[72:75], a[24:27], v[224:227]\n\t"
    "s_add_u32 m0, s27, 32768\n\t"
    "v_mfma_f32_16x16x32_bf16 v[240:243], a[72:75], a[28:31], v[240:243]\n\t"
    "buffer_load_dwordx4 v12, s[48:51], s78 offen offset:0 lds\n\t"
    "v_mfma_f32_16x16x32_bf16 v[244:247], a[76:79], a[28:31], v[244:247]\n\t"
    "v_mfma_f32_16x16x32_bf16 v[228:231], a[76:79], a[24:27], v[228:231]\n\t"
    "s_waitcnt vmcnt(22) lgkmcnt(0)\n\t"
    "s_barrier\n\t"
    "v_mfma_f32_16x16x32_bf16 v[128:131], a[88:91], a[4:7], v[128:131]\n\t"
    "ds_read_b64_tr_b16 a[72:73], v24 offset:0\n\t"
    "v_mfma_f32_16x16x32_bf16 v[144:147], a[88:91], a[8:11], v[144:147]\n\t"
    "ds_read_b128 a[36:39], v17 offset:32768\n\t"
    "v_mfma_f32_16x16x32_bf16 v[148:151], a[92:95], a[8:11], v[148:151]\n\t"
    "ds_read_b64_tr_b16 a[74:75], v24 offset:1024\n\t"
    "v_mfma_f32_16x16x32_bf16 v[132:135], a[92:95], a[4:7], v[132:135]\n\t"
    "ds_read_b128 a[40:43], v17 offset:34816\n\t"
    "v_mfma_f32_16x16x32_bf16 v[136:139], a[96:99], a[4:7], v[136:139]\n\t"
    "buffer_load_dwordx4 v13, s[48:51], s78 offen offset:1024 lds\n\t"
    "v_mfma_f32_16x16x32_bf16 v[152:155], a[96:99], a[8:11], v[152:155]\n\t"
    "ds_read_b64_tr_b16 a[76:77], v25 offset:0\n\t"
    "v_mfma_f32_16x16x32_bf16 v[156:159], a[100:103], a[8:11], v[156:159]\n\t"
    "ds_read_b128 a[44:47], v17 offset:36864\n\t"
    "v_mfma_f32_16x16x32_bf16 v[140:143], a[100:103], a[4:7], v[140:143]\n\t"
    "ds_read_b64_tr_b16 a[78:79], v25 offset:1024\n\t"
    "v_mfma_f32_16x16x32_bf16 v[168:171], a[96:99], a[12:15], v[168:171]\n\t"
    "ds_read_b128 a[48:51], v17 offset:38912\n\t"
    "v_mfma_f32_16x16x32_bf16 v[184:187], a[96:99], a[16:19], v[184:187]\n\t"
    "ds_read_b64_tr_b16 a[80:81], v26 offset:0\n\t"
    "v_mfma_f32_16x16x32_bf16 v[188:191], a[100:103], a[16:19], v[188:191]\n\t"
    "ds_read_b128 a[52:55], v17 offset:40960\n\t"
    "v_mfma_f32_16x16x32_bf16 v[172:175], a[100:103], a[12:15], v[172:175]\n\t"
    "ds_read_b64_tr_b16 a[82:83], v26 offset:1024\n\t"
    "v_mfma_f32_16x16x32_bf16 v[160:163], a[88:91], a[12:15], v[160:163]\n\t"
    "buffer_load_dwordx4 v14, s[48:51], s78 offen offset:2048 lds\n\t"
    "v_mfma_f32_16x16x32_bf16 v[176:179], a[88:91], a[16:19], v[176:179]\n\t"
    "ds_read_b128 a[56:59], v17 offset:43008\n\t"
    "v_mfma_f32_16x16x32_bf16 v[180:183], a[92:95], a[16:19], v[180:183]\n\t"
    "ds_read_b64_tr_b16 a[84:85], v27 offset:0\n\t"
    "v_mfma_f32_16x16x32_bf16 v[164:167], a[92:95], a[12:15], v[164:167]\n\t"
    "ds_read_b128 a[60:63], v17 offset:45056\n\t"
    "v_mfma_f32_16x16x32_bf16 v[192:195], a[88:91], a[20:23], v[192:195]\n\t"
    "ds_read_b64_tr_b16 a[86:87], v27 offset:1024\n\t"
    "v_mfma_f32_16x16x32_bf16 v[208:211], a[88:91], a[24:27], v[208:211]\n\t"
    "ds_read_b128 a[64:67], v17 offset:47104\n\t"
    "v_mfma_f32_16x16x32_bf16 v[212:215], a[92:95], a[24:27], v[212:215]\n\t"
    "v_mfma_f32_16x16x32_bf16 v[196:199], a[92:95], a[20:23], v[196:199]\n\t"
    "v_mfma_f32_16x16x32_bf16 v[200:203], a[96:99], a[20:23], v[200:203]\n\t"
    "buffer_load_dwordx4 v15, s[48:51], s78 offen offset:3072 lds\n\t"
    "v_mfma_f32_16x16x32_bf16 v[216:219], a[96:99], a[24:27], v[216:219]\n\t"
    "v_mfma_f32_16x16x32_bf16 v[220:223], a[100:103], a[24:27], v[220:223]\n\t"
    "v_mfma_f32_16x16x32_bf16 v[204:207], a[100:103], a[20:23], v[204:207]\n\t"
    "v_mfma_f32_16x16x32_bf16 v[232:235], a[96:99], a[28:31], v[232:235]\n\t"
    "v_mfma_f32_16x16x32_bf16 v[248:251], a[96:99], a[32:35], v[248:251]\n\t"
    "v_mfma_f32_16x16x32_bf16 v[252:255], a[100:103], a[32:35], v[252:255]\n\t"
    "v_mfma_f32_16x16x32_bf16 v[236:239], a[100:103], a[28:31], v[236:239]\n\t"
    "s_add_u32 m0, s27, 49152\n\t"
    "v_mfma_f32_16x16x32_bf16 v[224:227], a[88:91], a[28:31], v[224:227]\n\t"
    "buffer_load_dwordx4 v8, s[48:51], s78 offen offset:0 lds\n\t"
    "v_mfma_f32_16x16x32_bf16 v[240:243], a[88:91], a[32:35], v[240:243]\n\t"
    "v_mfma_f32_16x16x32_bf16 v[244:247], a[92:95], a[32:35], v[244:247]\n\t"
    "v_mfma_f32_16x16x32_bf16 v[228:231], a[92:95], a[28:31], v[228:231]\n\t"
    "s_waitcnt lgkmcnt(0)\n\t"
    "v_mfma_f32_16x16x32_bf16 v[128:131], a[72:75], a[36:39], v[128:131]\n\t"
    "ds_read_b64_tr_b16 a[88:89], v24 offset:8192\n\t"
    "v_mfma_f32_16x16x32_bf16 v[144:147], a[72:75], a[40:43], v[144:147]\n\t"
    "v_mfma_f32_16x16x32_bf16 v[148:151], a[76:79], a[40:43], v[148:151]\n\t"
    "ds_read_b128 a[68:71], v21 offset:32768\n\t"
    "v_mfma_f32_16x16x32_bf16 v[132:135], a[76:79], a[36:39], v[132:135]\n\t"
    "v_mfma_f32_16x16x32_bf16 v[136:139], a[80:83], a[36:39], v[136:139]\n\t"
    "buffer_load_dwordx4 v9, s[48:51], s78 offen offset:1024 lds\n\t"
    "v_mfma_f32_16x16x32_bf16 v[152:155], a[80:83], a[40:43], v[152:155]\n\t"
    "ds_read_b64_tr_b16 a[90:91], v24 offset:9216\n\t"
    "v_mfma_f32_16x16x32_bf16 v[156:159], a[84:87], a[40:43], v[156:159]\n\t"
    "v_mfma_f32_16x16x32_bf16 v[140:143], a[84:87], a[36:39], v[140:143]\n\t"
    "ds_read_b64_tr_b16 a[92:93], v25 offset:8192\n\t"
    "v_mfma_f32_16x16x32_bf16 v[168:171], a[80:83], a[44:47], v[168:171]\n\t"
    "v_mfma_f32_16x16x32_bf16 v[184:187], a[80:83], a[48:51], v[184:187]\n\t"
    "ds_read_b64_tr_b16 a[94:95], v25 offset:9216\n\t"
    "v_mfma_f32_16x16x32_bf16 v[188:191], a[84:87], a[48:51], v[188:191]\n\t"
    "v_mfma_f32_16x16x32_bf16 v[172:175], a[84:87], a[44:47], v[172:175]\n\t"
    "ds_read_b64_tr_b16 a[96:97], v26 offset:8192\n\t"
    "v_mfma_f32_16x16x32_bf16 v[160:163], a[72:75], a[44:47], v[160:163]\n\t"
    "buffer_load_dwordx4 v10, s[48:51], s78 offen offset:2048 lds\n\t"
    "v_mfma_f32_16x16x32_bf16 v[176:179], a[72:75], a[48:51], v[176:179]\n\t"
    "ds_read_b64_tr_b16 a[98:99], v26 offset:9216\n\t"
    "v_mfma_f32_16x16x32_bf16 v[180:183], a[76:79], a[48:51], v[180:183]\n\t"
    "v_mfma_f32_16x16x32_bf16 v[164:167], a[76:79], a[44:47], v[164:167]\n\t"
    "ds_read_b64_tr_b16 a[100:101], v27 offset:8192\n\t"
    "v_mfma_f32_16x16x32_bf16 v[192:195], a[72:75], a[52:55], v[192:195]\n\t"
    "v_mfma_f32_16x16x32_bf16 v[208:211], a[72:75], a[56:59], v[208:211]\n\t"
    "ds_read_b64_tr_b16 a[102:103], v27 offset:9216\n\t"
    "v_mfma_f32_16x16x32_bf16 v[212:215], a[76:79], a[56:59], v[212:215]\n\t"
    "v_mfma_f32_16x16x32_bf16 v[196:199], a[76:79], a[52:55], v[196:199]\n\t"
    "v_mfma_f32_16x16x32_bf16 v[200:203], a[80:83], a[52:55], v[200:203]\n\t"
    "buffer_load_dwordx4 v11, s[48:51], s78 offen offset:3072 lds\n\t"
    "v_mfma_f32_16x16x32_bf16 v[216:219], a[80:83], a[56:59], v[216:219]\n\t"
    "v_mfma_f32_16x16x32_bf16 v[220:223], a[84:87], a[56:59], v[220:223]\n\t"
    "v_mfma_f32_16x16x32_bf16 v[204:207], a[84:87], a[52:55], v[204:207]\n\t"
    "v_mfma_f32_16x16x32_bf16 v[232:235], a[80:83], a[60:63], v[232:235]\n\t"
    "v_mfma_f32_16x16x32_bf16 v[248:251], a[80:83], a[64:67], v[248:251]\n\t"
    "v_mfma_f32_16x16x32_bf16 v[252:255], a[84:87], a[64:67], v[252:255]\n\t"
    "v_mfma_f32_16x16x32_bf16 v[236:239], a[84:87], a[60:63], v[236:239]\n\t"
    "s_sub_u32 s6, s34, 16384\n\t"
    "s_subb_u32 s7, s35, 0\n\t"
    "v_mfma_f32_16x16x32_bf16 v[224:227], a[72:75], a[60:63], v[224:227]\n\t"
    "global_load_dword v59, v60, s[6:7]\n\t"
    "v_mfma_f32_16x16x32_bf16 v[240:243], a[72:75], a[64:67], v[240:243]\n\t"
    "v_mfma_f32_16x16x32_bf16 v[244:247], a[76:79], a[64:67], v[244:247]\n\t"
    "v_mfma_f32_16x16x32_bf16 v[228:231], a[76:79], a[60:63], v[228:231]\n\t"
    "s_waitcnt vmcnt(17) lgkmcnt(0)\n\t"
    "s_barrier\n\t"
    "v_mfma_f32_16x16x32_bf16 v[128:131], a[88:91], a[40:43], v[128:131]\n\t"
    "ds_read_b64_tr_b16 a[72:73], v24 offset:16384\n\t"
    "v_mfma_f32_16x16x32_bf16 v[144:147], a[88:91], a[44:47], v[144:147]\n\t"
    "ds_read_b128 a[0:3], v18 offset:0\n\t"
    "v_mfma_f32_16x16x32_bf16 v[148:151], a[92:95], a[44:47], v[148:151]\n\t"
    "s_add_u32 m0, s36, 32768\n\t"
    "ds_read_b64_tr_b16 a[74:75], v24 offset:17408\n\t"
    "v_mfma_f32_16x16x32_bf16 v[132:135], a[92:95], a[40:43], v[132:135]\n\t"
    "buffer_load_dwordx4 v64, s[40:43], s70 offen offset:0 lds\n\t"
    "v_mfma_f32_16x16x32_bf16 v[136:139], a[96:99], a[40:43], v[136:139]\n\t"
    "ds_read_b128 a[4:7], v18 offset:2048\n\t"
    "v_mfma_f32_16x16x32_bf16 v[152:155], a[96:99], a[44:47], v[152:155]\n\t"
    "ds_read_b64_tr_b16 a[76:77], v25 offset:16384\n\t"
    "v_mfma_f32_16x16x32_bf16 v[156:159], a[100:103], a[44:47], v[156:159]\n\t"
    "ds_read_b128 a[8:11], v18 offset:4096\n\t"
    "v_mfma_f32_16x16x32_bf16 v[140:143], a[100:103], a[40:43], v[140:143]\n\t"
    "ds_read_b64_tr_b16 a[78:79], v25 offset:17408\n\t"
    "v_mfma_f32_16x16x32_bf16 v[168:171], a[96:99], a[48:51], v[168:171]\n\t"
    "buffer_load_dwordx4 v65, s[40:43], s70 offen offset:1024 lds\n\t"
    "v_mfma_f32_16x16x32_bf16 v[184:187], a[96:99], a[52:55], v[184:187]\n\t"
    "ds_read_b128 a[12:15], v18 offset:6144\n\t"
    "v_mfma_f32_16x16x32_bf16 v[188:191], a[100:103], a[52:55], v[188:191]\n\t"
    "ds_read_b64_tr_b16 a[80:81], v26 offset:16384\n\t"
    "v_mfma_f32_16x16x32_bf16 v[172:175], a[100:103], a[48:51], v[172:175]\n\t"
    "ds_read_b128 a[16:19], v18 offset:8192\n\t"
    "v_mfma_f32_16x16x32_bf16 v[160:163], a[88:91], a[48:51], v[160:163]\n\t"
    "s_add_u32 m0, s37, 32768\n\t"
    "ds_read_b64_tr_b16 a[82:83], v26 offset:17408\n\t"
    "v_mfma_f32_16x16x32_bf16 v[176:179], a[88:91], a[52:55], v[176:179]\n\t"
    "buffer_load_dwordx4 v66, s[40:43], s70 offen offset:2048 lds\n\t"
    "v_mfma_f32_16x16x32_bf16 v[180:183], a[92:95], a[52:55], v[180:183]\n\t"
    "ds_read_b128 a[20:23], v18 offset:10240\n\t"
    "v_mfma_f32_16x16x32_bf16 v[164:167], a[92:95], a[48:51], v[164:167]\n\t"
    "ds_read_b64_tr_b16 a[84:85], v27 offset:16384\n\t"
    "v_mfma_f32_16x16x32_bf16 v[192:195], a[88:91], a[56:59], v[192:195]\n\t"
    "ds_read_b128 a[24:27], v18 offset:12288\n\t"
    "v_mfma_f32_16x16x32_bf16 v[208:211], a[88:91], a[60:63], v[208:211]\n\t"
    "ds_read_b64_tr_b16 a[86:87], v27 offset:17408\n\t"
    "v_mfma_f32_16x16x32_bf16 v[212:215], a[92:95], a[60:63], v[212:215]\n\t"
    "ds_read_b128 a[28:31], v18 offset:14336\n\t"
    "v_mfma_f32_16x16x32_bf16 v[196:199], a[92:95], a[56:59], v[196:199]\n\t"
    "buffer_load_dwordx4 v67, s[40:43], s70 offen offset:3072 lds\n\t"
    "v_mfma_f32_16x16x32_bf16 v[200:203], a[96:99], a[56:59], v[200:203]\n\t"
    "v_mfma_f32_16x16x32_bf16 v[216:219], a[96:99], a[60:63], v[216:219]\n\t"
    "v_mfma_f32_16x16x32_bf16 v[220:223], a[100:103], a[60:63], v[220:223]\n\t"
    "v_mfma_f32_16x16x32_bf16 v[204:207], a[100:103], a[56:59], v[204:207]\n\t"
    "s_add_u32 m0, s38, 32768\n\t"
    "v_mfma_f32_16x16x32_bf16 v[232:235], a[96:99], a[64:67], v[232:235]\n\t"
    "buffer_load_dwordx4 v68, s[40:43], s70 offen offset:0 lds\n\t"
    "v_mfma_f32_16x16x32_bf16 v[248:251], a[96:99], a[68:71], v[248:251]\n\t"
    "v_mfma_f32_16x16x32_bf16 v[252:255], a[100:103], a[68:71], v[252:255]\n\t"
    "v_mfma_f32_16x16x32_bf16 v[236:239], a[100:103], a[64:67], v[236:239]\n\t"
    "v_mfma_f32_16x16x32_bf16 v[224:227], a[88:91], a[64:67], v[224:227]\n\t"
    "v_mfma_f32_16x16x32_bf16 v[240:243], a[88:91], a[68:71], v[240:243]\n\t"
    "buffer_load_dwordx4 v69, s[40:43], s70 offen offset:1024 lds\n\t"
    "v_mfma_f32_16x16x32_bf16 v[244:247], a[92:95], a[68:71], v[244:247]\n\t"
    "v_mfma_f32_16x16x32_bf16 v[228:231], a[92:95], a[64:67], v[228:231]\n\t"
    "s_waitcnt lgkmcnt(0)\n\t"
    "v_mfma_f32_16x16x32_bf16 v[128:131], a[72:75], a[0:3], v[128:131]\n\t"
    "ds_read_b64_tr_b16 a[88:89], v24 offset:24576\n\t"
    "v_mfma_f32_16x16x32_bf16 v[144:147], a[72:75], a[4:7], v[144:147]\n\t"
    "ds_read_b128 a[36:39], v19 offset:0\n\t"
    "v_mfma_f32_16x16x32_bf16 v[148:151], a[76:79], a[4:7], v[148:151]\n\t"
    "s_add_u32 m0, s39, 32768\n\t"
    "ds_read_b64_tr_b16 a[90:91], v24 offset:25600\n\t"
    "v_mfma_f32_16x16x32_bf16 v[132:135], a[76:79], a[0:3], v[132:135]\n\t"
    "buffer_load_dwordx4 v70, s[40:43], s70 offen offset:2048 lds\n\t"
    "v_mfma_f32_16x16x32_bf16 v[136:139], a[80:83], a[0:3], v[136:139]\n\t"
    "ds_read_b128 a[40:43], v19 offset:2048\n\t"
    "v_mfma_f32_16x16x32_bf16 v[152:155], a[80:83], a[4:7], v[152:155]\n\t"
    "ds_read_b64_tr_b16 a[92:93], v25 offset:24576\n\t"
    "v_mfma_f32_16x16x32_bf16 v[156:159], a[84:87], a[4:7], v[156:159]\n\t"
    "ds_read_b128 a[44:47], v19 offset:4096\n\t"
    "v_mfma_f32_16x16x32_bf16 v[140:143], a[84:87], a[0:3], v[140:143]\n\t"
    "ds_read_b64_tr_b16 a[94:95], v25 offset:25600\n\t"
    "v_mfma_f32_16x16x32_bf16 v[168:171], a[80:83], a[8:11], v[168:171]\n\t"
    "ds_read_b128 a[48:51], v19 offset:6144\n\t"
    "v_mfma_f32_16x16x32_bf16 v[184:187], a[80:83], a[12:15], v[184:187]\n\t"
    "ds_read_b64_tr_b16 a[96:97], v26 offset:24576\n\t"
    "v_mfma_f32_16x16x32_bf16 v[188:191], a[84:87], a[12:15], v[188:191]\n\t"
    "buffer_load_dwordx4 v71, s[40:43], s70 offen offset:3072 lds\n\t"
    "v_mfma_f32_16x16x32_bf16 v[172:175], a[84:87], a[8:11], v[172:175]\n\t"
    "ds_read_b128 a[52:55], v19 offset:8192\n\t"
    "v_mfma_f32_16x16x32_bf16 v[160:163], a[72:75], a[8:11], v[160:163]\n\t"
    "ds_read_b64_tr_b16 a[98:99], v26 offset:25600\n\t"
    "v_mfma_f32_16x16x32_bf16 v[176:179], a[72:75], a[12:15], v[176:179]\n\t"
    "ds_read_b128 a[56:59], v19 offset:10240\n\t"
    "v_mfma_f32_16x16x32_bf16 v[180:183], a[76:79], a[12:15], v[180:183]\n\t"
    "ds_read_b64_tr_b16 a[100:101], v27 offset:24576\n\t"
    "v_mfma_f32_16x16x32_bf16 v[164:167], a[76:79], a[8:11], v[164:167]\n\t"
    "s_add_u32 m0, s27, 0\n\t"
    "ds_read_b128 a[60:63], v19 offset:12288\n\t"
    "v_mfma_f32_16x16x32_bf16 v[192:195], a[72:75], a[16:19], v[192:195]\n\t"
    "buffer_load_dwordx4 v8, s[52:55], s78 offen offset:0 lds\n\t"
    "v_mfma_f32_16x16x32_bf16 v[208:211], a[72:75], a[20:23], v[208:211]\n\t"
    "ds_read_b64_tr_b16 a[102:103], v27 offset:25600\n\t"
    "v_mfma_f32_16x16x32_bf16 v[212:215], a[76:79], a[20:23], v[212:215]\n\t"
    "ds_read_b128 a[64:67], v19 offset:14336\n\t"
    "v_mfma_f32_16x16x32_bf16 v[196:199], a[76:79], a[16:19], v[196:199]\n\t"
    "v_mfma_f32_16x16x32_bf16 v[200:203], a[80:83], a[16:19], v[200:203]\n\t"
    "v_mfma_f32_16x16x32_bf16 v[216:219], a[80:83], a[20:23], v[216:219]\n\t"
    "v_mfma_f32_16x16x32_bf16 v[220:223], a[84:87], a[20:23], v[220:223]\n\t"
    "v_mfma_f32_16x16x32_bf16 v[204:207], a[84:87], a[16:19], v[204:207]\n\t"
    "buffer_load_dwordx4 v9, s[52:55], s78 offen offset:1024 lds\n\t"
    "v_mfma_f32_16x16x32_bf16 v[232:235], a[80:83], a[24:27], v[232:235]\n\t"
    "v_mfma_f32_16x16x32_bf16 v[248:251], a[80:83], a[28:31], v[248:251]\n\t"
    "v_mfma_f32_16x16x32_bf16 v[252:255], a[84:87], a[28:31], v[252:255]\n\t"
    "v_mfma_f32_16x16x32_bf16 v[236:239], a[84:87], a[24:27], v[236:239]\n\t"
    "v_mfma_f32_16x16x32_bf16 v[224:227], a[72:75], a[24:27], v[224:227]\n\t"
    "v_mfma_f32_16x16x32_bf16 v[240:243], a[72:75], a[28:31], v[240:243]\n\t"
    "buffer_load_dwordx4 v10, s[52:55], s78 offen offset:2048 lds\n\t"
    "v_mfma_f32_16x16x32_bf16 v[244:247], a[76:79], a[28:31], v[244:247]\n\t"
    "v_mfma_f32_16x16x32_bf16 v[228:231], a[76:79], a[24:27], v[228:231]\n\t"
    "s_waitcnt vmcnt(16) lgkmcnt(0)\n\t"
    "s_barrier\n\t"
    "v_mfma_f32_16x16x32_bf16 v[128:131], a[88:91], a[36:39], v[128:131]\n\t"
    "ds_read_b64_tr_b16 a[72:73], v24 offset:32768\n\t"
    "v_mfma_f32_16x16x32_bf16 v[144:147], a[88:91], a[40:43], v[144:147]\n\t"
    "ds_read_b128 a[0:3], v16 offset:0\n\t"
    "v_mfma_f32_16x16x32_bf16 v[148:151], a[92:95], a[40:43], v[148:151]\n\t"
    "ds_read_b64_tr_b16 a[74:75], v24 offset:33792\n\t"
    "v_mfma_f32_16x16x32_bf16 v[132:135], a[92:95], a[36:39], v[132:135]\n\t"
    "buffer_load_dwordx4 v11, s[52:55], s78 offen offset:3072 lds\n\t"
    "v_mfma_f32_16x16x32_bf16 v[136:139], a[96:99], a[36:39], v[136:139]\n\t"
    "ds_read_b128 a[4:7], v16 offset:2048\n\t"
    "v_mfma_f32_16x16x32_bf16 v[152:155], a[96:99], a[40:43], v[152:155]\n\t"
    "ds_read_b64_tr_b16 a[76:77], v25 offset:32768\n\t"
    "v_mfma_f32_16x16x32_bf16 v[156:159], a[100:103], a[40:43], v[156:159]\n\t"
    "ds_read_b128 a[8:11], v16 offset:4096\n\t"
    "v_mfma_f32_16x16x32_bf16 v[140:143], a[100:103], a[36:39], v[140:143]\n\t"
    "s_add_u32 m0, s36, 65536\n\t"
    "ds_read_b64_tr_b16 a[78:79], v25 offset:33792\n\t"
    "v_mfma_f32_16x16x32_bf16 v[168:171], a[96:99], a[44:47], v[168:171]\n\t"
    "buffer_load_dwordx4 v64, s[40:43], s71 offen offset:0 lds\n\t"
    "v_mfma_f32_16x16x32_bf16 v[184:187], a[96:99], a[48:51], v[184:187]\n\t"
    "ds_read_b128 a[12:15], v16 offset:6144\n\t"
    "v_mfma_f32_16x16x32_bf16 v[188:191], a[100:103], a[48:51], v[188:191]\n\t"
    "ds_read_b64_tr_b16 a[80:81], v26 offset:32768\n\t"
    "v_mfma_f32_16x16x32_bf16 v[172:175], a[100:103], a[44:47], v[172:175]\n\t"
    "ds_read_b128 a[16:19], v16 offset:8192\n\t"
    "v_mfma_f32_16x16x32_bf16 v[160:163], a[88:91], a[44:47], v[160:163]\n\t"
    "ds_read_b64_tr_b16 a[82:83], v26 offset:33792\n\t"
    "v_mfma_f32_16x16x32_bf16 v[176:179], a[88:91], a[48:51], v[176:179]\n\t"
    "buffer_load_dwordx4 v65, s[40:43], s71 offen offset:1024 lds\n\t"
    "v_mfma_f32_16x16x32_bf16 v[180:183], a[92:95], a[48:51], v[180:183]\n\t"
    "ds_read_b128 a[20:23], v16 offset:10240\n\t"
    "v_mfma_f32_16x16x32_bf16 v[164:167], a[92:95], a[44:47], v[164:167]\n\t"
    "ds_read_b64_tr_b16 a[84:85], v27 offset:32768\n\t"
    "v_mfma_f32_16x16x32_bf16 v[192:195], a[88:91], a[52:55], v[192:195]\n\t"
    "ds_read_b128 a[24:27], v16 offset:12288\n\t"
    "v_mfma_f32_16x16x32_bf16 v[208:211], a[88:91], a[56:59], v[208:211]\n\t"
    "ds_read_b64_tr_b16 a[86:87], v27 offset:33792\n\t"
    "v_mfma_f32_16x16x32_bf16 v[212:215], a[92:95], a[56:59], v[212:215]\n\t"
    "s_add_u32 m0, s37, 65536\n\t"
    "ds_read_b128 a[28:31], v16 offset:14336\n\t"
    "v_mfma_f32_16x16x32_bf16 v[196:199], a[92:95], a[52:55], v[196:199]\n\t"
    "buffer_load_dwordx4 v66, s[40:43], s71 offen offset:2048 lds\n\t"
    "v_mfma_f32_16x16x32_bf16 v[200:203], a[96:99], a[52:55], v[200:203]\n\t"
    "v_mfma_f32_16x16x32_bf16 v[216:219], a[96:99], a[56:59], v[216:219]\n\t"
    "v_mfma_f32_16x16x32_bf16 v[220:223], a[100:103], a[56:59], v[220:223]\n\t"
    "v_mfma_f32_16x16x32_bf16 v[204:207], a[100:103], a[52:55], v[204:207]\n\t"
    "v_mfma_f32_16x16x32_bf16 v[232:235], a[96:99], a[60:63], v[232:235]\n\t"
    "buffer_load_dwordx4 v67, s[40:43], s71 offen offset:3072 lds\n\t"
    "v_mfma_f32_16x16x32_bf16 v[248:251], a[96:99], a[64:67], v[248:251]\n\t"
    "v_mfma_f32_16x16x32_bf16 v[252:255], a[100:103], a[64:67], v[252:255]\n\t"
    "v_mfma_f32_16x16x32_bf16 v[236:239], a[100:103], a[60:63], v[236:239]\n\t"
    "v_mfma_f32_16x16x32_bf16 v[224:227], a[88:91], a[60:63], v[224:227]\n\t"
    "s_add_u32 m0, s38, 65536\n\t"
    "v_mfma_f32_16x16x32_bf16 v[240:243], a[88:91], a[64:67], v[240:243]\n\t"
    "buffer_load_dwordx4 v68, s[40:43], s71 offen offset:0 lds\n\t"
    "v_mfma_f32_16x16x32_bf16 v[244:247], a[92:95], a[64:67], v[244:247]\n\t"
    "v_mfma_f32_16x16x32_bf16 v[228:231], a[92:95], a[60:63], v[228:231]\n\t"
    "s_waitcnt lgkmcnt(0)\n\t"
    "v_mfma_f32_16x16x32_bf16 v[128:131], a[72:75], a[0:3], v[128:131]\n\t"
    "ds_read_b64_tr_b16 a[88:89], v24 offset:40960\n\t"
    "v_mfma_f32_16x16x32_bf16 v[144:147], a[72:75], a[4:7], v[144:147]\n\t"
    "ds_read_b128 a[36:39], v17 offset:0\n\t"
    "v_mfma_f32_16x16x32_bf16 v[148:151], a[76:79], a[4:7], v[148:151]\n\t"
    "ds_read_b64_tr_b16 a[90:91], v24 offset:41984\n\t"
    "v_mfma_f32_16x16x32_bf16 v[132:135], a[76:79], a[0:3], v[132:135]\n\t"
    "buffer_load_dwordx4 v69, s[40:43], s71 offen offset:1024 lds\n\t"
    "v_mfma_f32_16x16x32_bf16 v[136:139], a[80:83], a[0:3], v[136:139]\n\t"
    "ds_read_b128 a[40:43], v17 offset:2048\n\t"
    "v_mfma_f32_16x16x32_bf16 v[152:155], a[80:83], a[4:7], v[152:155]\n\t"
    "ds_read_b64_tr_b16 a[92:93], v25 offset:40960\n\t"
    "v_mfma_f32_16x16x32_bf16 v[156:159], a[84:87], a[4:7], v[156:159]\n\t"
    "ds_read_b128 a[44:47], v17 offset:4096\n\t"
    "v_mfma_f32_16x16x32_bf16 v[140:143], a[84:87], a[0:3], v[140:143]\n\t"
    "s_add_u32 m0, s39, 65536\n\t"
    "ds_read_b64_tr_b16 a[94:95], v25 offset:41984\n\t"
    "v_mfma_f32_16x16x32_bf16 v[168:171], a[80:83], a[8:11], v[168:171]\n\t"
    "buffer_load_dwordx4 v70, s[40:43], s71 offen offset:2048 lds\n\t"
    "v_mfma_f32_16x16x32_bf16 v[184:187], a[80:83], a[12:15], v[184:187]\n\t"
    "ds_read_b128 a[48:51], v17 offset:6144\n\t"
    "v_mfma_f32_16x16x32_bf16 v[188:191], a[84:87], a[12:15], v[188:191]\n\t"
    "ds_read_b64_tr_b16 a[96:97], v26 offset:40960\n\t"
    "v_mfma_f32_16x16x32_bf16 v[172:175], a[84:87], a[8:11], v[172:175]\n\t"
    "ds_read_b128 a[52:55], v17 offset:8192\n\t"
    "v_mfma_f32_16x16x32_bf16 v[160:163], a[72:75], a[8:11], v[160:163]\n\t"
    "ds_read_b64_tr_b16 a[98:99], v26 offset:41984\n\t"
    "v_mfma_f32_16x16x32_bf16 v[176:179], a[72:75], a[12:15], v[176:179]\n\t"
    "buffer_load_dwordx4 v71, s[40:43], s71 offen offset:3072 lds\n\t"
    "v_mfma_f32_16x16x32_bf16 v[180:183], a[76:79], a[12:15], v[180:183]\n\t"
    "ds_read_b128 a[56:59], v17 offset:10240\n\t"
    "v_mfma_f32_16x16x32_bf16 v[164:167], a[76:79], a[8:11], v[164:167]\n\t"
    "ds_read_b64_tr_b16 a[100:101], v27 offset:40960\n\t"
    "v_mfma_f32_16x16x32_bf16 v[192:195], a[72:75], a[16:19], v[192:195]\n\t"
    "ds_read_b128 a[60:63], v17 offset:12288\n\t"
    "v_mfma_f32_16x16x32_bf16 v[208:211], a[72:75], a[20:23], v[208:211]\n\t"
    "ds_read_b64_tr_b16 a[102:103], v27 offset:41984\n\t"
    "v_mfma_f32_16x16x32_bf16 v[212:215], a[76:79], a[20:23], v[212:215]\n\t"
    "s_add_u32 m0, s27, 16384\n\t"
    "ds_read_b128 a[64:67], v17 offset:14336\n\t"
    "v_mfma_f32_16x16x32_bf16 v[196:199], a[76:79], a[16:19], v[196:199]\n\t"
    "buffer_load_dwordx4 v8, s[48:51], s79 offen offset:0 lds\n\t"
    "v_mfma_f32_16x16x32_bf16 v[200:203], a[80:83], a[16:19], v[200:203]\n\t"
    "v_mfma_f32_16x16x32_bf16 v[216:219], a[80:83], a[20:23], v[216:219]\n\t"
    "v_mfma_f32_16x16x32_bf16 v[220:223], a[84:87], a[20:23], v[220:223]\n\t"
    "v_mfma_f32_16x16x32_bf16 v[204:207], a[84:87], a[16:19], v[204:207]\n\t"
    "v_mfma_f32_16x16x32_bf16 v[232:235], a[80:83], a[24:27], v[232:235]\n\t"
    "buffer_load_dwordx4 v9, s[48:51], s79 offen offset:1024 lds\n\t"
    "v_mfma_f32_16x16x32_bf16 v[248:251], a[80:83], a[28:31], v[248:251]\n\t"
    "v_mfma_f32_16x16x32_bf16 v[252:255], a[84:87], a[28:31], v[252:255]\n\t"
    "v_mfma_f32_16x16x32_bf16 v[236:239], a[84:87], a[24:27], v[236:239]\n\t"
    "v_mfma_f32_16x16x32_bf16 v[224:227], a[72:75], a[24:27], v[224:227]\n\t"
    "s_sub_u32 s6, s34, 8192\n\t"
    "s_subb_u32 s7, s35, 0\n\t"
    "v_mfma_f32_16x16x32_bf16 v[240:243], a[72:75], a[28:31], v[240:243]\n\t"
    "global_load_dword v59, v60, s[6:7]\n\t"
    "v_mfma_f32_16x16x32_bf16 v[244:247], a[76:79], a[28:31], v[244:247]\n\t"
    "v_mfma_f32_16x16x32_bf16 v[228:231], a[76:79], a[24:27], v[228:231]\n\t"
    "s_waitcnt vmcnt(15) lgkmcnt(0)\n\t"
    "s_barrier\n\t"
    "v_mfma_f32_16x16x32_bf16 v[128:131], a[88:91], a[36:39], v[128:131]\n\t"
    "ds_read_b64_tr_b16 a[72:73], v24 offset:49152\n\t"
    "v_mfma_f32_16x16x32_bf16 v[144:147], a[88:91], a[40:43], v[144:147]\n\t"
    "ds_read_b128 a[0:3], v16 offset:32768\n\t"
    "v_mfma_f32_16x16x32_bf16 v[148:151], a[92:95], a[40:43], v[148:151]\n\t"
    "ds_read_b64_tr_b16 a[74:75], v24 offset:50176\n\t"
    "v_mfma_f32_16x16x32_bf16 v[132:135], a[92:95], a[36:39], v[132:135]\n\t"
    "buffer_load_dwordx4 v10, s[48:51], s79 offen offset:2048 lds\n\t"
    "v_mfma_f32_16x16x32_bf16 v[136:139], a[96:99], a[36:39], v[136:139]\n\t"
    "ds_read_b128 a[4:7], v16 offset:34816\n\t"
    "v_mfma_f32_16x16x32_bf16 v[152:155], a[96:99], a[40:43], v[152:155]\n\t"
    "ds_read_b64_tr_b16 a[76:77], v25 offset:49152\n\t"
    "v_mfma_f32_16x16x32_bf16 v[156:159], a[100:103], a[40:43], v[156:159]\n\t"
    "ds_read_b128 a[8:11], v16 offset:36864\n\t"
    "v_mfma_f32_16x16x32_bf16 v[140:143], a[100:103], a[36:39], v[140:143]\n\t"
    "ds_read_b64_tr_b16 a[78:79], v25 offset:50176\n\t"
    "v_mfma_f32_16x16x32_bf16 v[168:171], a[96:99], a[44:47], v[168:171]\n\t"
    "buffer_load_dwordx4 v11, s[48:51], s79 offen offset:3072 lds\n\t"
    "v_mfma_f32_16x16x32_bf16 v[184:187], a[96:99], a[48:51], v[184:187]\n\t"
    "ds_read_b128 a[12:15], v16 offset:38912\n\t"
    "v_mfma_f32_16x16x32_bf16 v[188:191], a[100:103], a[48:51], v[188:191]\n\t"
    "ds_read_b64_tr_b16 a[80:81], v26 offset:49152\n\t"
    "v_mfma_f32_16x16x32_bf16 v[172:175], a[100:103], a[44:47], v[172:175]\n\t"
    "ds_read_b128 a[16:19], v16 offset:40960\n\t"
    "v_mfma_f32_16x16x32_bf16 v[160:163], a[88:91], a[44:47], v[160:163]\n\t"
    "s_add_u32 m0, s27, 32768\n\t"
    "ds_read_b64_tr_b16 a[82:83], v26 offset:50176\n\t"
    "v_mfma_f32_16x16x32_bf16 v[176:179], a[88:91], a[48:51], v[176:179]\n\t"
    "buffer_load_dwordx4 v8, s[52:55], s79 offen offset:0 lds\n\t"
    "v_mfma_f32_16x16x32_bf16 v[180:183], a[92:95], a[48:51], v[180:183]\n\t"
    "ds_read_b128 a[20:23], v16 offset:43008\n\t"
    "v_mfma_f32_16x16x32_bf16 v[164:167], a[92:95], a[44:47], v[164:167]\n\t"
    "ds_read_b64_tr_b16 a[84:85], v27 offset:49152\n\t"
    "v_mfma_f32_16x16x32_bf16 v[192:195], a[88:91], a[52:55], v[192:195]\n\t"
    "ds_read_b128 a[24:27], v16 offset:45056\n\t"
    "v_mfma_f32_16x16x32_bf16 v[208:211], a[88:91], a[56:59], v[208:211]\n\t"
    "ds_read_b64_tr_b16 a[86:87], v27 offset:50176\n\t"
    "v_mfma_f32_16x16x32_bf16 v[212:215], a[92:95], a[56:59], v[212:215]\n\t"
    "ds_read_b128 a[28:31], v16 offset:47104\n\t"
    "v_mfma_f32_16x16x32_bf16 v[196:199], a[92:95], a[52:55], v[196:199]\n\t"
    "buffer_load_dwordx4 v9, s[52:55], s79 offen offset:1024 lds\n\t"
    "v_mfma_f32_16x16x32_bf16 v[200:203], a[96:99], a[52:55], v[200:203]\n\t"
    "v_mfma_f32_16x16x32_bf16 v[216:219], a[96:99], a[56:59], v[216:219]\n\t"
    "v_mfma_f32_16x16x32_bf16 v[220:223], a[100:103], a[56:59], v[220:223]\n\t"
    "v_mfma_f32_16x16x32_bf16 v[204:207], a[100:103], a[52:55], v[204:207]\n\t"
    "v_mfma_f32_16x16x32_bf16 v[232:235], a[96:99], a[60:63], v[232:235]\n\t"
    "buffer_load_dwordx4 v10, s[52:55], s79 offen offset:2048 lds\n\t"
    "v_mfma_f32_16x16x32_bf16 v[248:251], a[96:99], a[64:67], v[248:251]\n\t"
    "v_mfma_f32_16x16x32_bf16 v[252:255], a[100:103], a[64:67], v[252:255]\n\t"
    "v_mfma_f32_16x16x32_bf16 v[236:239], a[100:103], a[60:63], v[236:239]\n\t"
    "v_mfma_f32_16x16x32_bf16 v[224:227], a[88:91], a[60:63], v[224:227]\n\t"
    "v_mfma_f32_16x16x32_bf16 v[240:243], a[88:91], a[64:67], v[240:243]\n\t"
    "buffer_load_dwordx4 v11, s[52:55], s79 offen offset:3072 lds\n\t"
    "v_mfma_f32_16x16x32_bf16 v[244:247], a[92:95], a[64:67], v[244:247]\n\t"
    "v_mfma_f32_16x16x32_bf16 v[228:231], a[92:95], a[60:63], v[228:231]\n\t"
    "s_waitcnt lgkmcnt(0)\n\t"
    "v_mfma_f32_16x16x32_bf16 v[128:131], a[72:75], a[0:3], v[128:131]\n\t"
    "ds_read_b64_tr_b16 a[88:89], v24 offset:57344\n\t"
    "v_mfma_f32_16x16x32_bf16 v[144:147], a[72:75], a[4:7], v[144:147]\n\t"
    "v_mfma_f32_16x16x32_bf16 v[148:151], a[76:79], a[4:7], v[148:151]\n\t"
    "s_add_u32 m0, s36, 0\n\t"
    "ds_read_b128 a[32:35], v20 offset:32768\n\t"
    "v_mfma_f32_16x16x32_bf16 v[132:135], a[76:79], a[0:3], v[132:135]\n\t"
    "buffer_load_dwordx4 v64, s[44:47], s71 offen offset:0 lds\n\t"
    "v_mfma_f32_16x16x32_bf16 v[136:139], a[80:83], a[0:3], v[136:139]\n\t"
    "ds_read_b64_tr_b16 a[90:91], v24 offset:58368\n\t"
    "v_mfma_f32_16x16x32_bf16 v[152:155], a[80:83], a[4:7], v[152:155]\n\t"
    "v_mfma_f32_16x16x32_bf16 v[156:159], a[84:87], a[4:7], v[156:159]\n\t"
    "ds_read_b64_tr_b16 a[92:93], v25 offset:57344\n\t"
    "v_mfma_f32_16x16x32_bf16 v[140:143], a[84:87], a[0:3], v[140:143]\n\t"
    "v_mfma_f32_16x16x32_bf16 v[168:171], a[80:83], a[8:11], v[168:171]\n\t"
    "buffer_load_dwordx4 v65, s[44:47], s71 offen offset:1024 lds\n\t"
    "v_mfma_f32_16x16x32_bf16 v[184:187], a[80:83], a[12:15], v[184:187]\n\t"
    "ds_read_b64_tr_b16 a[94:95], v25 offset:58368\n\t"
    "v_mfma_f32_16x16x32_bf16 v[188:191], a[84:87], a[12:15], v[188:191]\n\t"
    "v_mfma_f32_16x16x32_bf16 v[172:175], a[84:87], a[8:11], v[172:175]\n\t"
    "ds_read_b64_tr_b16 a[96:97], v26 offset:57344\n\t"
    "v_mfma_f32_16x16x32_bf16 v[160:163], a[72:75], a[8:11], v[160:163]\n\t"
    "s_add_u32 m0, s37, 0\n\t"
    "v_mfma_f32_16x16x32_bf16 v[176:179], a[72:75], a[12:15], v[176:179]\n\t"
    "buffer_load_dwordx4 v66, s[44:47], s71 offen offset:2048 lds\n\t"
    "v_mfma_f32_16x16x32_bf16 v[180:183], a[76:79], a[12:15], v[180:183]\n\t"
    "ds_read_b64_tr_b16 a[98:99], v26 offset:58368\n\t"
    "v_mfma_f32_16x16x32_bf16 v[164:167], a[76:79], a[8:11], v[164:167]\n\t"
    "v_mfma_f32_16x16x32_bf16 v[192:195], a[72:75], a[16:19], v[192:195]\n\t"
    "ds_read_b64_tr_b16 a[100:101], v27 offset:57344\n\t"
    "v_mfma_f32_16x16x32_bf16 v[208:211], a[72:75], a[20:23], v[208:211]\n\t"
    "v_mfma_f32_16x16x32_bf16 v[212:215], a[76:79], a[20:23], v[212:215]\n\t"
    "ds_read_b64_tr_b16 a[102:103], v27 offset:58368\n\t"
    "v_mfma_f32_16x16x32_bf16 v[196:199], a[76:79], a[16:19], v[196:199]\n\t"
    "buffer_load_dwordx4 v67, s[44:47], s71 offen offset:3072 lds\n\t"
    "v_mfma_f32_16x16x32_bf16 v[200:203], a[80:83], a[16:19], v[200:203]\n\t"
    "v_mfma_f32_16x16x32_bf16 v[216:219], a[80:83], a[20:23], v[216:219]\n\t"
    "v_mfma_f32_16x16x32_bf16 v[220:223], a[84:87], a[20:23], v[220:223]\n\t"
    "v_mfma_f32_16x16x32_bf16 v[204:207], a[84:87], a[16:19], v[204:207]\n\t"
    "s_add_u32 m0, s38, 0\n\t"
    "v_mfma_f32_16x16x32_bf16 v[232:235], a[80:83], a[24:27], v[232:235]\n\t"
    "buffer_load_dwordx4 v68, s[44:47], s71 offen offset:0 lds\n\t"
    "v_mfma_f32_16x16x32_bf16 v[248:251], a[80:83], a[28:31], v[248:251]\n\t"
    "v_mfma_f32_16x16x32_bf16 v[252:255], a[84:87], a[28:31], v[252:255]\n\t"
    "v_mfma_f32_16x16x32_bf16 v[236:239], a[84:87], a[24:27], v[236:239]\n\t"
    "v_mfma_f32_16x16x32_bf16 v[224:227], a[72:75], a[24:27], v[224:227]\n\t"
    "v_mfma_f32_16x16x32_bf16 v[240:243], a[72:75], a[28:31], v[240:243]\n\t"
    "buffer_load_dwordx2 v[50:51], v31, s[60:63], 0 offen sc1\n\t"
    "v_mfma_f32_16x16x32_bf16 v[244:247], a[76:79], a[28:31], v[244:247]\n\t"
    "v_mfma_f32_16x16x32_bf16 v[228:231], a[76:79], a[24:27], v[228:231]\n\t"
    "s_waitcnt vmcnt(23) lgkmcnt(0)\n\t"
    "s_barrier\n\t"
    "v_mfma_f32_16x16x32_bf16 v[128:131], a[88:91], a[4:7], v[128:131]\n\t"
    "ds_read_b64_tr_b16 a[72:73], v24 offset:0\n\t"
    "v_mfma_f32_16x16x32_bf16 v[144:147], a[88:91], a[8:11], v[144:147]\n\t"
    "ds_read_b128 a[36:39], v17 offset:32768\n\t"
    "v_mfma_f32_16x16x32_bf16 v[148:151], a[92:95], a[8:11], v[148:151]\n\t"
    "buffer_load_dwordx4 v69, s[44:47], s71 offen offset:1024 lds\n\t"
    "v_mfma_f32_16x16x32_bf16 v[132:135], a[92:95], a[4:7], v[132:135]\n\t"
    "ds_read_b64_tr_b16 a[74:75], v24 offset:1024\n\t"
    "v_mfma_f32_16x16x32_bf16 v[136:139], a[96:99], a[4:7], v[136:139]\n\t"
    "ds_read_b128 a[40:43], v17 offset:34816\n\t"
    "v_mfma_f32_16x16x32_bf16 v[152:155], a[96:99], a[8:11], v[152:155]\n\t"
    "ds_read_b64_tr_b16 a[76:77], v25 offset:0\n\t"
    "v_mfma_f32_16x16x32_bf16 v[156:159], a[100:103], a[8:11], v[156:159]\n\t"
    "s_add_u32 m0, s39, 0\n\t"
    "ds_read_b128 a[44:47], v17 offset:36864\n\t"
    "v_mfma_f32_16x16x32_bf16 v[140:143], a[100:103], a[4:7], v[140:143]\n\t"
    "buffer_load_dwordx4 v70, s[44:47], s71 offen offset:2048 lds\n\t"
    "v_mfma_f32_16x16x32_bf16 v[168:171], a[96:99], a[12:15], v[168:171]\n\t"
    "ds_read_b64_tr_b16 a[78:79], v25 offset:1024\n\t"
    "v_mfma_f32_16x16x32_bf16 v[184:187], a[96:99], a[16:19], v[184:187]\n\t"
    "ds_read_b128 a[48:51], v17 offset:38912\n\t"
    "v_mfma_f32_16x16x32_bf16 v[188:191], a[100:103], a[16:19], v[188:191]\n\t"
    "ds_read_b64_tr_b16 a[80:81], v26 offset:0\n\t"
    "v_mfma_f32_16x16x32_bf16 v[172:175], a[100:103], a[12:15], v[172:175]\n\t"
    "buffer_load_dwordx4 v71, s[44:47], s71 offen offset:3072 lds\n\t"
    "v_mfma_f32_16x16x32_bf16 v[160:163], a[88:91], a[12:15], v[160:163]\n\t"
    "ds_read_b128 a[52:55], v17 offset:40960\n\t"
    "v_mfma_f32_16x16x32_bf16 v[176:179], a[88:91], a[16:19], v[176:179]\n\t"
    "ds_read_b64_tr_b16 a[82:83], v26 offset:1024\n\t"
    "v_mfma_f32_16x16x32_bf16 v[180:183], a[92:95], a[16:19], v[180:183]\n\t"
    "ds_read_b128 a[56:59], v17 offset:43008\n\t"
    "v_mfma_f32_16x16x32_bf16 v[164:167], a[92:95], a[12:15], v[164:167]\n\t"
    "s_add_u32 m0, s27, 49152\n\t"
    "ds_read_b64_tr_b16 a[84:85], v27 offset:0\n\t"
    "v_mfma_f32_16x16x32_bf16 v[192:195], a[88:91], a[20:23], v[192:195]\n\t"
    "buffer_load_dwordx4 v12, s[48:51], s79 offen offset:0 lds\n\t"
    "v_mfma_f32_16x16x32_bf16 v[208:211], a[88:91], a[24:27], v[208:211]\n\t"
    "ds_read_b128 a[60:63], v17 offset:45056\n\t"
    "v_mfma_f32_16x16x32_bf16 v[212:215], a[92:95], a[24:27], v[212:215]\n\t"
    "ds_read_b64_tr_b16 a[86:87], v27 offset:1024\n\t"
    "v_mfma_f32_16x16x32_bf16 v[196:199], a[92:95], a[20:23], v[196:199]\n\t"
    "ds_read_b128 a[64:67], v17 offset:47104\n\t"
    "v_mfma_f32_16x16x32_bf16 v[200:203], a[96:99], a[20:23], v[200:203]\n\t"
    "v_mfma_f32_16x16x32_bf16 v[216:219], a[96:99], a[24:27], v[216:219]\n\t"
    "buffer_load_dwordx4 v13, s[48:51], s79 offen offset:1024 lds\n\t"
    "v_mfma_f32_16x16x32_bf16 v[220:223], a[100:103], a[24:27], v[220:223]\n\t"
    "v_mfma_f32_16x16x32_bf16 v[204:207], a[100:103], a[20:23], v[204:207]\n\t"
    "v_mfma_f32_16x16x32_bf16 v[232:235], a[96:99], a[28:31], v[232:235]\n\t"
    "v_mfma_f32_16x16x32_bf16 v[248:251], a[96:99], a[32:35], v[248:251]\n\t"
    "buffer_load_dwordx4 v14, s[48:51], s79 offen offset:2048 lds\n\t"
    "v_mfma_f32_16x16x32_bf16 v[252:255], a[100:103], a[32:35], v[252:255]\n\t"
    "v_mfma_f32_16x16x32_bf16 v[236:239], a[100:103], a[28:31], v[236:239]\n\t"
    "v_mfma_f32_16x16x32_bf16 v[224:227], a[88:91], a[28:31], v[224:227]\n\t"
    "v_mfma_f32_16x16x32_bf16 v[240:243], a[88:91], a[32:35], v[240:243]\n\t"
    "v_mfma_f32_16x16x32_bf16 v[244:247], a[92:95], a[32:35], v[244:247]\n\t"
    "buffer_load_dwordx4 v15, s[48:51], s79 offen offset:3072 lds\n\t"
    "v_mfma_f32_16x16x32_bf16 v[228:231], a[92:95], a[28:31], v[228:231]\n\t"
    "s_waitcnt lgkmcnt(0)\n\t"
    "v_mfma_f32_16x16x32_bf16 v[128:131], a[72:75], a[36:39], v[128:131]\n\t"
    "ds_read_b64_tr_b16 a[88:89], v24 offset:8192\n\t"
    "v_mfma_f32_16x16x32_bf16 v[144:147], a[72:75], a[40:43], v[144:147]\n\t"
    "v_mfma_f32_16x16x32_bf16 v[148:151], a[76:79], a[40:43], v[148:151]\n\t"
    "ds_read_b128 a[68:71], v21 offset:32768\n\t"
    "v_mfma_f32_16x16x32_bf16 v[132:135], a[76:79], a[36:39], v[132:135]\n\t"
    "v_mfma_f32_16x16x32_bf16 v[136:139], a[80:83], a[36:39], v[136:139]\n\t"
    "ds_read_b64_tr_b16 a[90:91], v24 offset:9216\n\t"
    "v_mfma_f32_16x16x32_bf16 v[152:155], a[80:83], a[40:43], v[152:155]\n\t"
    "v_mfma_f32_16x16x32_bf16 v[156:159], a[84:87], a[40:43], v[156:159]\n\t"
    "ds_read_b64_tr_b16 a[92:93], v25 offset:8192\n\t"
    "v_mfma_f32_16x16x32_bf16 v[140:143], a[84:87], a[36:39], v[140:143]\n\t"
    "v_mfma_f32_16x16x32_bf16 v[168:171], a[80:83], a[44:47], v[168:171]\n\t"
    "ds_read_b64_tr_b16 a[94:95], v25 offset:9216\n\t"
    "v_mfma_f32_16x16x32_bf16 v[184:187], a[80:83], a[48:51], v[184:187]\n\t"
    "v_mfma_f32_16x16x32_bf16 v[188:191], a[84:87], a[48:51], v[188:191]\n\t"
    "ds_read_b64_tr_b16 a[96:97], v26 offset:8192\n\t"
    "v_mfma_f32_16x16x32_bf16 v[172:175], a[84:87], a[44:47], v[172:175]\n\t"
    "v_mfma_f32_16x16x32_bf16 v[160:163], a[72:75], a[44:47], v[160:163]\n\t"
    "ds_read_b64_tr_b16 a[98:99], v26 offset:9216\n\t"
    "v_mfma_f32_16x16x32_bf16 v[176:179], a[72:75], a[48:51], v[176:179]\n\t"
    "v_mfma_f32_16x16x32_bf16 v[180:183], a[76:79], a[48:51], v[180:183]\n\t"
    "ds_read_b64_tr_b16 a[100:101], v27 offset:8192\n\t"
    "v_mfma_f32_16x16x32_bf16 v[164:167], a[76:79], a[44:47], v[164:167]\n\t"
    "v_mfma_f32_16x16x32_bf16 v[192:195], a[72:75], a[52:55], v[192:195]\n\t"
    "ds_read_b64_tr_b16 a[102:103], v27 offset:9216\n\t"
    "v_mfma_f32_16x16x32_bf16 v[208:211], a[72:75], a[56:59], v[208:211]\n\t"
    "v_mfma_f32_16x16x32_bf16 v[212:215], a[76:79], a[56:59], v[212:215]\n\t"
    "v_mfma_f32_16x16x32_bf16 v[196:199], a[76:79], a[52:55], v[196:199]\n\t"
    "v_mfma_f32_16x16x32_bf16 v[200:203], a[80:83], a[52:55], v[200:203]\n\t"
    "v_mfma_f32_16x16x32_bf16 v[216:219], a[80:83], a[56:59], v[216:219]\n\t"
    "v_mfma_f32_16x16x32_bf16 v[220:223], a[84:87], a[56:59], v[220:223]\n\t"
    "v_mfma_f32_16x16x32_bf16 v[204:207], a[84:87], a[52:55], v[204:207]\n\t"
    "v_mfma_f32_16x16x32_bf16 v[232:235], a[80:83], a[60:63], v[232:235]\n\t"
    "v_mfma_f32_16x16x32_bf16 v[248:251], a[80:83], a[64:67], v[248:251]\n\t"
    "v_mfma_f32_16x16x32_bf16 v[252:255], a[84:87], a[64:67], v[252:255]\n\t"
    "v_mfma_f32_16x16x32_bf16 v[236:239], a[84:87], a[60:63], v[236:239]\n\t"
    "v_mfma_f32_16x16x32_bf16 v[224:227], a[72:75], a[60:63], v[224:227]\n\t"
    "v_mfma_f32_16x16x32_bf16 v[240:243], a[72:75], a[64:67], v[240:243]\n\t"
    "v_mfma_f32_16x16x32_bf16 v[244:247], a[76:79], a[64:67], v[244:247]\n\t"
    "v_mfma_f32_16x16x32_bf16 v[228:231], a[76:79], a[60:63], v[228:231]\n\t"
    "s_waitcnt vmcnt(13) lgkmcnt(0)\n\t"
    "s_barrier\n\t"
    "v_mfma_f32_16x16x32_bf16 v[128:131], a[88:91], a[40:43], v[128:131]\n\t"
    "ds_read_b64_tr_b16 a[104:105], v24 offset:16384\n\t"
    "ds_read_b64_tr_b16 a[106:107], v24 offset:17408\n\t"
    "v_mfma_f32_16x16x32_bf16 v[144:147], a[88:91], a[44:47], v[144:147]\n\t"
    "ds_read_b128 a[152:155], v18 offset:0\n\t"
    "s_xor_b32 s8, s83, 0\n\t"
    "v_mfma_f32_16x16x32_bf16 v[148:151], a[92:95], a[44:47], v[148:151]\n\t"
    "ds_read_b64_tr_b16 a[108:109], v25 offset:16384\n\t"
    "ds_read_b64_tr_b16 a[110:111], v25 offset:17408\n\t"
    "v_mfma_f32_16x16x32_bf16 v[132:135], a[92:95], a[40:43], v[132:135]\n\t"
    "ds_read_b128 a[156:159], v18 offset:2048\n\t"
    "s_xor_b32 s9, s83, 4096\n\t"
    "v_mfma_f32_16x16x32_bf16 v[136:139], a[96:99], a[40:43], v[136:139]\n\t"
    "ds_read_b64_tr_b16 a[116:117], v25 offset:24576\n\t"
    "ds_read_b64_tr_b16 a[118:119], v25 offset:25600\n\t"
    "v_mfma_f32_16x16x32_bf16 v[152:155], a[96:99], a[44:47], v[152:155]\n\t"
    "ds_read_b128 a[188:191], v19 offset:0\n\t"
    "s_xor_b32 s10, s83, 8192\n\t"
    "v_mfma_f32_16x16x32_bf16 v[156:159], a[100:103], a[44:47], v[156:159]\n\t"
    "ds_read_b64_tr_b16 a[112:113], v24 offset:24576\n\t"
    "ds_read_b64_tr_b16 a[114:115], v24 offset:25600\n\t"
    "v_mfma_f32_16x16x32_bf16 v[140:143], a[100:103], a[40:43], v[140:143]\n\t"
    "ds_read_b128 a[192:195], v19 offset:2048\n\t"
    "s_xor_b32 s11, s83, 12288\n\t"
    "v_mfma_f32_16x16x32_bf16 v[168:171], a[96:99], a[48:51], v[168:171]\n\t"
    "ds_read_b64_tr_b16 a[120:121], v24 offset:32768\n\t"
    "ds_read_b64_tr_b16 a[122:123], v24 offset:33792\n\t"
    "v_mfma_f32_16x16x32_bf16 v[184:187], a[96:99], a[52:55], v[184:187]\n\t"
    "ds_read_b128 a[160:163], v18 offset:4096\n\t"
    "s_xor_b32 s12, s83, 16384\n\t"
    "v_mfma_f32_16x16x32_bf16 v[188:191], a[100:103], a[52:55], v[188:191]\n\t"
    "ds_read_b64_tr_b16 a[124:125], v25 offset:32768\n\t"
    "ds_read_b64_tr_b16 a[126:127], v25 offset:33792\n\t"
    "v_mfma_f32_16x16x32_bf16 v[172:175], a[100:103], a[48:51], v[172:175]\n\t"
    "ds_read_b128 a[196:199], v19 offset:4096\n\t"
    "s_xor_b32 s13, s83, 20480\n\t"
    "v_mfma_f32_16x16x32_bf16 v[160:163], a[88:91], a[48:51], v[160:163]\n\t"
    "ds_read_b64_tr_b16 a[132:133], v25 offset:40960\n\t"
    "ds_read_b64_tr_b16 a[134:135], v25 offset:41984\n\t"
    "v_mfma_f32_16x16x32_bf16 v[176:179], a[88:91], a[52:55], v[176:179]\n\t"
    "ds_read_b128 a[164:167], v18 offset:6144\n\t"
    "s_xor_b32 s14, s83, 24576\n\t"
    "v_mfma_f32_16x16x32_bf16 v[180:183], a[92:95], a[52:55], v[180:183]\n\t"
    "ds_read_b64_tr_b16 a[128:129], v24 offset:40960\n\t"
    "ds_read_b64_tr_b16 a[130:131], v24 offset:41984\n\t"
    "v_mfma_f32_16x16x32_bf16 v[164:167], a[92:95], a[48:51], v[164:167]\n\t"
    "ds_read_b128 a[200:203], v19 offset:6144\n\t"
    "s_xor_b32 s15, s83, 28672\n\t"
    "v_mfma_f32_16x16x32_bf16 v[192:195], a[88:91], a[56:59], v[192:195]\n\t"
    "v_mfma_f32_16x16x32_bf16 v[208:211], a[88:91], a[60:63], v[208:211]\n\t"
    "v_mfma_f32_16x16x32_bf16 v[212:215], a[92:95], a[60:63], v[212:215]\n\t"
    "v_mfma_f32_16x16x32_bf16 v[196:199], a[92:95], a[56:59], v[196:199]\n\t"
    "v_mfma_f32_16x16x32_bf16 v[200:203], a[96:99], a[56:59], v[200:203]\n\t"
    "v_mfma_f32_16x16x32_bf16 v[216:219], a[96:99], a[60:63], v[216:219]\n\t"
    "v_mfma_f32_16x16x32_bf16 v[220:223], a[100:103], a[60:63], v[220:223]\n\t"
    "v_mfma_f32_16x16x32_bf16 v[204:207], a[100:103], a[56:59], v[204:207]\n\t"
    "v_mfma_f32_16x16x32_bf16 v[232:235], a[96:99], a[64:67], v[232:235]\n\t"
    "v_mfma_f32_16x16x32_bf16 v[248:251], a[96:99], a[68:71], v[248:251]\n\t"
    "v_mfma_f32_16x16x32_bf16 v[252:255], a[100:103], a[68:71], v[252:255]\n\t"
    "v_mfma_f32_16x16x32_bf16 v[236:239], a[100:103], a[64:67], v[236:239]\n\t"
    "v_mfma_f32_16x16x32_bf16 v[224:227], a[88:91], a[64:67], v[224:227]\n\t"
    "v_mfma_f32_16x16x32_bf16 v[240:243], a[88:91], a[68:71], v[240:243]\n\t"
    "v_mfma_f32_16x16x32_bf16 v[244:247], a[92:95], a[68:71], v[244:247]\n\t"
    "v_mfma_f32_16x16x32_bf16 v[228:231], a[92:95], a[64:67], v[228:231]\n\t"
    "s_waitcnt lgkmcnt(0)\n\t"
    "v_mfma_f32_16x16x32_bf16 v[128:131], a[104:107], a[152:155], v[128:131]\n\t"
    "ds_read_b128 a[168:171], v18 offset:8192\n\t"
    "v_mbcnt_lo_u32_b32 v97, -1, 0\n\t"
    "v_mfma_f32_16x16x32_bf16 v[132:135], a[108:111], a[152:155], v[132:135]\n\t"
    "ds_read_b128 a[204:207], v19 offset:8192\n\t"
    "v_mov_b32 v96, 0\n\t"
    "v_mfma_f32_16x16x32_bf16 v[132:135], a[116:119], a[156:159], v[132:135]\n\t"
    "ds_read_b128 a[172:175], v18 offset:10240\n\t"
    "v_and_b32 v97, 15, v97\n\t"
    "v_mfma_f32_16x16x32_bf16 v[128:131], a[112:115], a[156:159], v[128:131]\n\t"
    "ds_read_b128 a[208:211], v19 offset:10240\n\t"
    "v_lshl_add_u32 v97, v97, 2, v47\n\t"
    "v_mfma_f32_16x16x32_bf16 v[128:131], a[120:123], a[188:191], v[128:131]\n\t"
    "ds_read_b128 a[176:179], v18 offset:12288\n\t"
    "ds_write_b32 v97, v96\n\t"
    "v_mfma_f32_16x16x32_bf16 v[132:135], a[124:127], a[188:191], v[132:135]\n\t"
    "ds_read_b128 a[212:215], v19 offset:12288\n\t"
    "v_mfma_f32_16x16x32_bf16 v[132:135], a[132:135], a[192:195], v[132:135]\n\t"
    "ds_read_b128 a[180:183], v18 offset:14336\n\t"
    "v_mfma_f32_16x16x32_bf16 v[128:131], a[128:131], a[192:195], v[128:131]\n\t"
    "ds_read_b128 a[216:219], v19 offset:14336\n\t"
    "v_mfma_f32_16x16x32_bf16 v[144:147], a[104:107], a[156:159], v[144:147]\n\t"
    "s_waitcnt vmcnt(0)\n\t"
    "s_barrier\n\t"
    "v_mfma_f32_16x16x32_bf16 v[148:151], a[108:111], a[156:159], v[148:151]\n\t"
    "ds_read_b64_tr_b16 a[136:137], v24 offset:49152\n\t"
    "ds_read_b64_tr_b16 a[138:139], v24 offset:50176\n\t"
    "v_mfma_f32_16x16x32_bf16 v[148:151], a[116:119], a[160:163], v[148:151]\n\t"
    "ds_read_b128 a[0:3], v16 offset:0\n\t"
    "v_mfma_f32_16x16x32_bf16 v[144:147], a[112:115], a[160:163], v[144:147]\n\t"
    "ds_read_b64_tr_b16 a[140:141], v25 offset:49152\n\t"
    "s_waitcnt lgkmcnt(6)\n\t"
    "v_mfma_f32_16x16x32_bf16 v[144:147], a[120:123], a[192:195], v[144:147]\n\t"
    "ds_read_b64_tr_b16 a[142:143], v25 offset:50176\n\t"
    "ds_read_b64_tr_b16 a[148:149], v25 offset:57344\n\t"
    "v_mfma_f32_16x16x32_bf16 v[148:151], a[124:127], a[192:195], v[148:151]\n\t"
    "ds_read_b64_tr_b16 a[150:151], v25 offset:58368\n\t"
    "ds_read_b64_tr_b16 a[144:145], v24 offset:57344\n\t"
    "v_mfma_f32_16x16x32_bf16 v[148:151], a[132:135], a[196:199], v[148:151]\n\t"
    "ds_read_b128 a[32:35], v17 offset:0\n\t"
    "v_mfma_f32_16x16x32_bf16 v[144:147], a[128:131], a[196:199], v[144:147]\n\t"
    "ds_read_b64_tr_b16 a[146:147], v24 offset:58368\n\t"
    "ds_read_b64_tr_b16 a[224:225], v26 offset:16384\n\t"
    "v_mfma_f32_16x16x32_bf16 v[160:163], a[104:107], a[160:163], v[160:163]\n\t"
    "s_waitcnt lgkmcnt(6)\n\t"
    "v_mfma_f32_16x16x32_bf16 v[164:167], a[108:111], a[160:163], v[164:167]\n\t"
    "ds_read_b128 a[4:7], v16 offset:2048\n\t"
    "v_mfma_f32_16x16x32_bf16 v[164:167], a[116:119], a[164:167], v[164:167]\n\t"
    "ds_read_b128 a[36:39], v17 offset:2048\n\t"
    "v_mfma_f32_16x16x32_bf16 v[160:163], a[112:115], a[164:167], v[160:163]\n\t"
    "ds_read_b128 a[8:11], v16 offset:4096\n\t"
    "v_mfma_f32_16x16x32_bf16 v[160:163], a[120:123], a[196:199], v[160:163]\n\t"
    "ds_read_b128 a[40:43], v17 offset:4096\n\t"
    "v_mfma_f32_16x16x32_bf16 v[164:167], a[124:127], a[196:199], v[164:167]\n\t"
    "ds_read_b128 a[12:15], v16 offset:6144\n\t"
    "v_mfma_f32_16x16x32_bf16 v[164:167], a[132:135], a[200:203], v[164:167]\n\t"
    "ds_read_b128 a[44:47], v17 offset:6144\n\t"
    "v_mfma_f32_16x16x32_bf16 v[160:163], a[128:131], a[200:203], v[160:163]\n\t"
    "ds_read_b128 a[16:19], v16 offset:8192\n\t"
    "s_waitcnt lgkmcnt(8)\n\t"
    "v_mfma_f32_16x16x32_bf16 v[128:131], a[136:139], a[0:3], v[128:131]\n\t"
    "ds_read_b128 a[48:51], v17 offset:8192\n\t"
    "v_mfma_f32_16x16x32_bf16 v[132:135], a[140:143], a[0:3], v[132:135]\n\t"
    "ds_read_b128 a[184:187], v22 offset:0\n\t"
    "v_mfma_f32_16x16x32_bf16 v[132:135], a[148:151], a[32:35], v[132:135]\n\t"
    "ds_read_b128 a[220:223], v23 offset:0\n\t"
    "v_mfma_f32_16x16x32_bf16 v[128:131], a[144:147], a[32:35], v[128:131]\n\t"
    "ds_read_b128 a[20:23], v16 offset:10240\n\t"
    "v_mfma_f32_16x16x32_bf16 v[176:179], a[104:107], a[164:167], v[176:179]\n\t"
    "ds_read_b128 a[52:55], v17 offset:10240\n\t"
    "s_waitcnt lgkmcnt(6)\n\t"
    "v_mfma_f32_16x16x32_bf16 v[180:183], a[108:111], a[164:167], v[180:183]\n\t"
    "ds_read_b128 a[24:27], v16 offset:12288\n\t"
    "v_mul_f32 v76, v132, v132\n\t"
    "v_mfma_f32_16x16x32_bf16 v[180:183], a[116:119], a[168:171], v[180:183]\n\t"
    "ds_read_b128 a[56:59], v17 offset:12288\n\t"
    "v_mul_f32 v77, v133, v133\n\t"
    "v_mfma_f32_16x16x32_bf16 v[176:179], a[112:115], a[168:171], v[176:179]\n\t"
    "ds_read_b128 a[28:31], v16 offset:14336\n\t"
    "v_mul_f32 v78, v134, v134\n\t"
    "v_mfma_f32_16x16x32_bf16 v[176:179], a[120:123], a[200:203], v[176:179]\n\t"
    "ds_read_b128 a[60:63], v17 offset:14336\n\t"
    "v_mul_f32 v79, v135, v135\n\t"
    "v_mfma_f32_16x16x32_bf16 v[180:183], a[124:127], a[200:203], v[180:183]\n\t"
    "ds_read_b64_tr_b16 a[226:227], v26 offset:17408\n\t"
    "ds_read_b64_tr_b16 a[228:229], v27 offset:16384\n\t"
    "v_mfma_f32_16x16x32_bf16 v[180:183], a[132:135], a[204:207], v[180:183]\n\t"
    "ds_read_b64_tr_b16 a[230:231], v27 offset:17408\n\t"
    "v_mul_f32 v72, v128, v128\n\t"
    "v_mfma_f32_16x16x32_bf16 v[176:179], a[128:131], a[204:207], v[176:179]\n\t"
    "v_mul_f32 v73, v129, v129\n\t"
    "v_mul_f32 v74, v130, v130\n\t"
    "v_mfma_f32_16x16x32_bf16 v[144:147], a[136:139], a[4:7], v[144:147]\n\t"
    "v_mul_f32 v75, v131, v131\n\t"
    "s_waitcnt lgkmcnt(6)\n\t"
    "v_mfma_f32_16x16x32_bf16 v[148:151], a[140:143], a[4:7], v[148:151]\n\t"
    "ds_read_b64_tr_b16 a[236:237], v27 offset:24576\n\t"
    "ds_read_b64_tr_b16 a[238:239], v27 offset:25600\n\t"
    "v_mfma_f32_16x16x32_bf16 v[148:151], a[148:151], a[36:39], v[148:151]\n\t"
    "ds_read_b64_tr_b16 a[232:233], v26 offset:24576\n\t"
    "ds_read_b64_tr_b16 a[234:235], v26 offset:25600\n\t"
    "v_mfma_f32_16x16x32_bf16 v[144:147], a[144:147], a[36:39], v[144:147]\n\t"
    "ds_read_b64_tr_b16 a[240:241], v26 offset:32768\n\t"
    "ds_read_b64_tr_b16 a[242:243], v26 offset:33792\n\t"
    "v_mfma_f32_16x16x32_bf16 v[192:195], a[104:107], a[168:171], v[192:195]\n\t"
    "ds_read_b64_tr_b16 a[244:245], v27 offset:32768\n\t"
    "s_waitcnt lgkmcnt(6)\n\t"
    "v_mfma_f32_16x16x32_bf16 v[196:199], a[108:111], a[168:171], v[196:199]\n\t"
    "ds_read_b64_tr_b16 a[246:247], v27 offset:33792\n\t"
    "ds_read_b64_tr_b16 a[252:253], v27 offset:40960\n\t"
    "v_mfma_f32_16x16x32_bf16 v[196:199], a[116:119], a[172:175], v[196:199]\n\t"
    "ds_read_b64_tr_b16 a[254:255], v27 offset:41984\n\t"
    "ds_read_b64_tr_b16 a[248:249], v26 offset:40960\n\t"
    "v_mfma_f32_16x16x32_bf16 v[192:195], a[112:115], a[172:175], v[192:195]\n\t"
    "ds_read_b64_tr_b16 a[250:251], v26 offset:41984\n\t"
    "ds_read_b64_tr_b16 a[64:65], v26 offset:49152\n\t"
    "v_mfma_f32_16x16x32_bf16 v[192:195], a[120:123], a[204:207], v[192:195]\n\t"
    "ds_read_b64_tr_b16 a[66:67], v26 offset:50176\n\t"
    "v_cndmask_b32 v148, 0, v148, s[28:29]\n\t"
    "v_mfma_f32_16x16x32_bf16 v[196:199], a[124:127], a[204:207], v[196:199]\n\t"
    "v_cndmask_b32 v149, 0, v149, s[28:29]\n\t"
    "v_cndmask_b32 v150, 0, v150, s[28:29]\n\t"
    "v_mfma_f32_16x16x32_bf16 v[196:199], a[132:135], a[208:211], v[196:199]\n\t"
    "v_cndmask_b32 v151, 0, v151, s[28:29]\n\t"
    "v_add_f32 v68, v132, v148\n\t"
    "v_mfma_f32_16x16x32_bf16 v[192:195], a[128:131], a[208:211], v[192:195]\n\t"
    "v_add_f32 v69, v133, v149\n\t"
    "v_add_f32 v70, v134, v150\n\t"
    "v_mfma_f32_16x16x32_bf16 v[160:163], a[136:139], a[8:11], v[160:163]\n\t"
    "v_add_f32 v71, v135, v151\n\t"
    "v_fmac_f32 v76, v148, v148\n\t"
    "v_mfma_f32_16x16x32_bf16 v[164:167], a[140:143], a[8:11], v[164:167]\n\t"
    "v_fmac_f32 v77, v149, v149\n\t"
    "v_fmac_f32 v78, v150, v150\n\t"
    "v_mfma_f32_16x16x32_bf16 v[164:167], a[148:151], a[40:43], v[164:167]\n\t"
    "v_fmac_f32 v79, v151, v151\n\t"
    "v_cndmask_b32 v144, 0, v144, s[28:29]\n\t"
    "v_mfma_f32_16x16x32_bf16 v[160:163], a[144:147], a[40:43], v[160:163]\n\t"
    "v_cndmask_b32 v145, 0, v145, s[28:29]\n\t"
    "v_cndmask_b32 v146, 0, v146, s[28:29]\n\t"
    "v_mfma_f32_16x16x32_bf16 v[208:211], a[104:107], a[172:175], v[208:211]\n\t"
    "v_cndmask_b32 v147, 0, v147, s[28:29]\n\t"
    "v_add_f32 v64, v128, v144\n\t"
    "v_mfma_f32_16x16x32_bf16 v[212:215], a[108:111], a[172:175], v[212:215]\n\t"
    "v_add_f32 v65, v129, v145\n\t"
    "v_add_f32 v66, v130, v146\n\t"
    "v_mfma_f32_16x16x32_bf16 v[212:215], a[116:119], a[176:179], v[212:215]\n\t"
    "v_add_f32 v67, v131, v147\n\t"
    "v_fmac_f32 v72, v144, v144\n\t"
    "v_mfma_f32_16x16x32_bf16 v[208:211], a[112:115], a[176:179], v[208:211]\n\t"
    "v_fmac_f32 v73, v145, v145\n\t"
    "v_fmac_f32 v74, v146, v146\n\t"
    "v_mfma_f32_16x16x32_bf16 v[208:211], a[120:123], a[208:211], v[208:211]\n\t"
    "v_fmac_f32 v75, v147, v147\n\t"
    "v_cndmask_b32 v164, 0, v164, s[30:31]\n\t"
    "v_mfma_f32_16x16x32_bf16 v[212:215], a[124:127], a[208:211], v[212:215]\n\t"
    "v_cndmask_b32 v165, 0, v165, s[30:31]\n\t"
    "v_cndmask_b32 v166, 0, v166, s[30:31]\n\t"
    "v_mfma_f32_16x16x32_bf16 v[212:215], a[132:135], a[212:215], v[212:215]\n\t"
    "v_cndmask_b32 v167, 0, v167, s[30:31]\n\t"
    "v_add_f32 v68, v68, v164\n\t"
    "v_mfma_f32_16x16x32_bf16 v[208:211], a[128:131], a[212:215], v[208:211]\n\t"
    "v_add_f32 v69, v69, v165\n\t"
    "v_add_f32 v70, v70, v166\n\t"
    "v_mfma_f32_16x16x32_bf16 v[176:179], a[136:139], a[12:15], v[176:179]\n\t"
    "v_add_f32 v71, v71, v167\n\t"
    "v_fmac_f32 v76, v164, v164\n\t"
    "v_mfma_f32_16x16x32_bf16 v[180:183], a[140:143], a[12:15], v[180:183]\n\t"
    "v_fmac_f32 v77, v165, v165\n\t"
    "v_fmac_f32 v78, v166, v166\n\t"
    "v_mfma_f32_16x16x32_bf16 v[180:183], a[148:151], a[44:47], v[180:183]\n\t"
    "v_fmac_f32 v79, v167, v167\n\t"
    "v_cndmask_b32 v160, 0, v160, s[30:31]\n\t"
    "v_mfma_f32_16x16x32_bf16 v[176:179], a[144:147], a[44:47], v[176:179]\n\t"
    "v_cndmask_b32 v161, 0, v161, s[30:31]\n\t"
    "v_cndmask_b32 v162, 0, v162, s[30:31]\n\t"
    "v_mfma_f32_16x16x32_bf16 v[224:227], a[104:107], a[176:179], v[224:227]\n\t"
    "v_cndmask_b32 v163, 0, v163, s[30:31]\n\t"
    "v_add_f32 v64, v64, v160\n\t"
    "v_mfma_f32_16x16x32_bf16 v[228:231], a[108:111], a[176:179], v[228:231]\n\t"
    "v_add_f32 v65, v65, v161\n\t"
    "v_add_f32 v66, v66, v162\n\t"
    "v_mfma_f32_16x16x32_bf16 v[228:231], a[116:119], a[180:183], v[228:231]\n\t"
    "v_add_f32 v67, v67, v163\n\t"
    "v_fmac_f32 v72, v160, v160\n\t"
    "v_mfma_f32_16x16x32_bf16 v[224:227], a[112:115], a[180:183], v[224:227]\n\t"
    "v_fmac_f32 v73, v161, v161\n\t"
    "v_fmac_f32 v74, v162, v162\n\t"
    "v_mfma_f32_16x16x32_bf16 v[224:227], a[120:123], a[212:215], v[224:227]\n\t"
    "v_fmac_f32 v75, v163, v163\n\t"
    "v_add_f32 v68, v68, v180\n\t"
    "v_mfma_f32_16x16x32_bf16 v[228:231], a[124:127], a[212:215], v[228:231]\n\t"
    "v_add_f32 v69, v69, v181\n\t"
    "v_add_f32 v70, v70, v182\n\t"
    "v_mfma_f32_16x16x32_bf16 v[228:231], a[132:135], a[216:219], v[228:231]\n\t"
    "v_add_f32 v71, v71, v183\n\t"
    "v_fmac_f32 v76, v180, v180\n\t"
    "v_mfma_f32_16x16x32_bf16 v[224:227], a[128:131], a[216:219], v[224:227]\n\t"
    "v_fmac_f32 v77, v181, v181\n\t"
    "v_fmac_f32 v78, v182, v182\n\t"
    "v_mfma_f32_16x16x32_bf16 v[192:195], a[136:139], a[16:19], v[192:195]\n\t"
    "v_fmac_f32 v79, v183, v183\n\t"
    "v_add_f32 v64, v64, v176\n\t"
    "v_mfma_f32_16x16x32_bf16 v[196:199], a[140:143], a[16:19], v[196:199]\n\t"
    "v_add_f32 v65, v65, v177\n\t"
    "v_add_f32 v66, v66, v178\n\t"
    "v_mfma_f32_16x16x32_bf16 v[196:199], a[148:151], a[48:51], v[196:199]\n\t"
    "v_add_f32 v67, v67, v179\n\t"
    "v_fmac_f32 v72, v176, v176\n\t"
    "v_mfma_f32_16x16x32_bf16 v[192:195], a[144:147], a[48:51], v[192:195]\n\t"
    "v_fmac_f32 v73, v177, v177\n\t"
    "v_fmac_f32 v74, v178, v178\n\t"
    "v_mfma_f32_16x16x32_bf16 v[240:243], a[104:107], a[180:183], v[240:243]\n\t"
    "v_fmac_f32 v75, v179, v179\n\t"
    "s_waitcnt lgkmcnt(6)\n\t"
    "v_mfma_f32_16x16x32_bf16 v[244:247], a[108:111], a[180:183], v[244:247]\n\t"
    "ds_read_b64_tr_b16 a[68:69], v27 offset:49152\n\t"
    "ds_read_b64_tr_b16 a[70:71], v27 offset:50176\n\t"
    "v_mfma_f32_16x16x32_bf16 v[244:247], a[116:119], a[184:187], v[244:247]\n\t"
    "ds_read_b64_tr_b16 a[76:77], v27 offset:57344\n\t"
    "ds_read_b64_tr_b16 a[78:79], v27 offset:58368\n\t"
    "v_mfma_f32_16x16x32_bf16 v[240:243], a[112:115], a[184:187], v[240:243]\n\t"
    "ds_read_b64_tr_b16 a[72:73], v26 offset:57344\n\t"
    "ds_read_b64_tr_b16 a[74:75], v26 offset:58368\n\t"
    "v_mfma_f32_16x16x32_bf16 v[240:243], a[120:123], a[216:219], v[240:243]\n\t"
    "v_add_f32 v68, v68, v196\n\t"
    "v_add_f32 v69, v69, v197\n\t"
    "v_mfma_f32_16x16x32_bf16 v[244:247], a[124:127], a[216:219], v[244:247]\n\t"
    "v_add_f32 v70, v70, v198\n\t"
    "v_add_f32 v71, v71, v199\n\t"
    "v_mfma_f32_16x16x32_bf16 v[244:247], a[132:135], a[220:223], v[244:247]\n\t"
    "v_fmac_f32 v76, v196, v196\n\t"
    "v_fmac_f32 v77, v197, v197\n\t"
    "v_mfma_f32_16x16x32_bf16 v[240:243], a[128:131], a[220:223], v[240:243]\n\t"
    "v_fmac_f32 v78, v198, v198\n\t"
    "v_fmac_f32 v79, v199, v199\n\t"
    "v_mfma_f32_16x16x32_bf16 v[208:211], a[136:139], a[20:23], v[208:211]\n\t"
    "v_add_f32 v64, v64, v192\n\t"
    "v_add_f32 v65, v65, v193\n\t"
    "v_mfma_f32_16x16x32_bf16 v[212:215], a[140:143], a[20:23], v[212:215]\n\t"
    "v_add_f32 v66, v66, v194\n\t"
    "v_add_f32 v67, v67, v195\n\t"
    "v_mfma_f32_16x16x32_bf16 v[212:215], a[148:151], a[52:55], v[212:215]\n\t"
    "v_fmac_f32 v72, v192, v192\n\t"
    "v_fmac_f32 v73, v193, v193\n\t"
    "v_mfma_f32_16x16x32_bf16 v[208:211], a[144:147], a[52:55], v[208:211]\n\t"
    "v_fmac_f32 v74, v194, v194\n\t"
    "v_fmac_f32 v75, v195, v195\n\t"
    "v_mfma_f32_16x16x32_bf16 v[224:227], a[136:139], a[24:27], v[224:227]\n\t"
    "v_mfma_f32_16x16x32_bf16 v[228:231], a[140:143], a[24:27], v[228:231]\n\t"
    "v_mfma_f32_16x16x32_bf16 v[228:231], a[148:151], a[56:59], v[228:231]\n\t"
    "v_add_f32 v68, v68, v212\n\t"
    "v_add_f32 v69, v69, v213\n\t"
    "v_mfma_f32_16x16x32_bf16 v[224:227], a[144:147], a[56:59], v[224:227]\n\t"
    "v_add_f32 v70, v70, v214\n\t"
    "v_add_f32 v71, v71, v215\n\t"
    "v_mfma_f32_16x16x32_bf16 v[240:243], a[136:139], a[28:31], v[240:243]\n\t"
    "v_fmac_f32 v76, v212, v212\n\t"
    "v_fmac_f32 v77, v213, v213\n\t"
    "v_mfma_f32_16x16x32_bf16 v[244:247], a[140:143], a[28:31], v[244:247]\n\t"
    "v_fmac_f32 v78, v214, v214\n\t"
    "v_fmac_f32 v79, v215, v215\n\t"
    "v_mfma_f32_16x16x32_bf16 v[244:247], a[148:151], a[60:63], v[244:247]\n\t"
    "v_add_f32 v64, v64, v208\n\t"
    "v_add_f32 v65, v65, v209\n\t"
    "v_mfma_f32_16x16x32_bf16 v[240:243], a[144:147], a[60:63], v[240:243]\n\t"
    "v_add_f32 v66, v66, v210\n\t"
    "s_waitcnt lgkmcnt(0)\n\t"
    "v_mfma_f32_16x16x32_bf16 v[136:139], a[224:227], a[152:155], v[136:139]\n\t"
    "v_add_f32 v67, v67, v211\n\t"
    "v_fmac_f32 v72, v208, v208\n\t"
    "v_mfma_f32_16x16x32_bf16 v[140:143], a[228:231], a[152:155], v[140:143]\n\t"
    "v_fmac_f32 v73, v209, v209\n\t"
    "v_fmac_f32 v74, v210, v210\n\t"
    "v_mfma_f32_16x16x32_bf16 v[140:143], a[236:239], a[156:159], v[140:143]\n\t"
    "v_fmac_f32 v75, v211, v211\n\t"
    "v_add_f32 v68, v68, v228\n\t"
    "v_mfma_f32_16x16x32_bf16 v[136:139], a[232:235], a[156:159], v[136:139]\n\t"
    "v_add_f32 v69, v69, v229\n\t"
    "v_add_f32 v70, v70, v230\n\t"
    "v_mfma_f32_16x16x32_bf16 v[136:139], a[240:243], a[188:191], v[136:139]\n\t"
    "v_add_f32 v71, v71, v231\n\t"
    "v_fmac_f32 v76, v228, v228\n\t"
    "v_mfma_f32_16x16x32_bf16 v[140:143], a[244:247], a[188:191], v[140:143]\n\t"
    "v_fmac_f32 v77, v229, v229\n\t"
    "v_fmac_f32 v78, v230, v230\n\t"
    "v_mfma_f32_16x16x32_bf16 v[140:143], a[252:255], a[192:195], v[140:143]\n\t"
    "v_fmac_f32 v79, v231, v231\n\t"
    "v_add_f32 v64, v64, v224\n\t"
    "v_mfma_f32_16x16x32_bf16 v[136:139], a[248:251], a[192:195], v[136:139]\n\t"
    "v_add_f32 v65, v65, v225\n\t"
    "v_add_f32 v66, v66, v226\n\t"
    "v_mfma_f32_16x16x32_bf16 v[136:139], a[64:67], a[0:3], v[136:139]\n\t"
    "v_add_f32 v67, v67, v227\n\t"
    "v_fmac_f32 v72, v224, v224\n\t"
    "v_mfma_f32_16x16x32_bf16 v[140:143], a[68:71], a[0:3], v[140:143]\n\t"
    "v_fmac_f32 v73, v225, v225\n\t"
    "v_fmac_f32 v74, v226, v226\n\t"
    "v_mfma_f32_16x16x32_bf16 v[140:143], a[76:79], a[32:35], v[140:143]\n\t"
    "v_fmac_f32 v75, v227, v227\n\t"
    "v_add_f32 v68, v68, v244\n\t"
    "v_mfma_f32_16x16x32_bf16 v[136:139], a[72:75], a[32:35], v[136:139]\n\t"
    "v_add_f32 v69, v69, v245\n\t"
    "v_add_f32 v70, v70, v246\n\t"
    "v_mfma_f32_16x16x32_bf16 v[152:155], a[224:227], a[156:159], v[152:155]\n\t"
    "v_add_f32 v71, v71, v247\n\t"
    "v_fmac_f32 v76, v244, v244\n\t"
    "v_mfma_f32_16x16x32_bf16 v[156:159], a[228:231], a[156:159], v[156:159]\n\t"
    "v_fmac_f32 v77, v245, v245\n\t"
    "v_fmac_f32 v78, v246, v246\n\t"
    "v_mfma_f32_16x16x32_bf16 v[156:159], a[236:239], a[160:163], v[156:159]\n\t"
    "v_fmac_f32 v79, v247, v247\n\t"
    "v_add_f32 v64, v64, v240\n\t"
    "v_mfma_f32_16x16x32_bf16 v[152:155], a[232:235], a[160:163], v[152:155]\n\t"
    "v_add_f32 v65, v65, v241\n\t"
    "v_add_f32 v66, v66, v242\n\t"
    "v_mfma_f32_16x16x32_bf16 v[152:155], a[240:243], a[192:195], v[152:155]\n\t"
    "v_add_f32 v67, v67, v243\n\t"
    "v_fmac_f32 v72, v240, v240\n\t"
    "v_mfma_f32_16x16x32_bf16 v[156:159], a[244:247], a[192:195], v[156:159]\n\t"
    "v_fmac_f32 v73, v241, v241\n\t"
    "v_fmac_f32 v74, v242, v242\n\t"
    "v_mfma_f32_16x16x32_bf16 v[156:159], a[252:255], a[196:199], v[156:159]\n\t"
    "v_fmac_f32 v75, v243, v243\n\t"
    "v_add_f32_dpp v64, v64, v64 row_ror:8 row_mask:0xf bank_mask:0x3\n\t"
    "v_mfma_f32_16x16x32_bf16 v[152:155], a[248:251], a[196:199], v[152:155]\n\t"
    "v_add_f32_dpp v65, v65, v65 row_ror:8 row_mask:0xf bank_mask:0x3\n\t"
    "v_add_f32_dpp v66, v66, v66 row_ror:8 row_mask:0xf bank_mask:0x3\n\t"
    "v_mfma_f32_16x16x32_bf16 v[152:155], a[64:67], a[4:7], v[152:155]\n\t"
    "v_add_f32_dpp v67, v67, v67 row_ror:8 row_mask:0xf bank_mask:0x3\n\t"
    "v_add_f32_dpp v68, v68, v68 row_ror:8 row_mask:0xf bank_mask:0x3\n\t"
    "v_mfma_f32_16x16x32_bf16 v[156:159], a[68:71], a[4:7], v[156:159]\n\t"
    "v_add_f32_dpp v69, v69, v69 row_ror:8 row_mask:0xf bank_mask:0x3\n\t"
    "v_add_f32_dpp v70, v70, v70 row_ror:8 row_mask:0xf bank_mask:0x3\n\t"
    "v_mfma_f32_16x16x32_bf16 v[156:159], a[76:79], a[36:39], v[156:159]\n\t"
    "v_add_f32_dpp v71, v71, v71 row_ror:8 row_mask:0xf bank_mask:0x3\n\t"
    "v_add_f32_dpp v64, v72, v72 row_ror:8 row_mask:0xf bank_mask:0xc\n\t"
    "v_mfma_f32_16x16x32_bf16 v[152:155], a[72:75], a[36:39], v[152:155]\n\t"
    "v_add_f32_dpp v65, v73, v73 row_ror:8 row_mask:0xf bank_mask:0xc\n\t"
    "v_add_f32_dpp v66, v74, v74 row_ror:8 row_mask:0xf bank_mask:0xc\n\t"
    "v_mfma_f32_16x16x32_bf16 v[168:171], a[224:227], a[160:163], v[168:171]\n\t"
    "v_add_f32_dpp v67, v75, v75 row_ror:8 row_mask:0xf bank_mask:0xc\n\t"
    "v_add_f32_dpp v68, v76, v76 row_ror:8 row_mask:0xf bank_mask:0xc\n\t"
    "v_mfma_f32_16x16x32_bf16 v[172:175], a[228:231], a[160:163], v[172:175]\n\t"
    "v_add_f32_dpp v69, v77, v77 row_ror:8 row_mask:0xf bank_mask:0xc\n\t"
    "v_add_f32_dpp v70, v78, v78 row_ror:8 row_mask:0xf bank_mask:0xc\n\t"
    "v_mfma_f32_16x16x32_bf16 v[172:175], a[236:239], a[164:167], v[172:175]\n\t"
    "v_add_f32_dpp v71, v79, v79 row_ror:8 row_mask:0xf bank_mask:0xc\n\t"
    "v_add_f32_dpp v64, v64, v64 row_half_mirror row_mask:0xf bank_mask:0x5\n\t"
    "v_mfma_f32_16x16x32_bf16 v[168:171], a[232:235], a[164:167], v[168:171]\n\t"
    "v_add_f32_dpp v65, v65, v65 row_half_mirror row_mask:0xf bank_mask:0x5\n\t"
    "v_add_f32_dpp v66, v66, v66 row_half_mirror row_mask:0xf bank_mask:0x5\n\t"
    "v_mfma_f32_16x16x32_bf16 v[168:171], a[240:243], a[196:199], v[168:171]\n\t"
    "v_add_f32_dpp v67, v67, v67 row_half_mirror row_mask:0xf bank_mask:0x5\n\t"
    "v_add_f32_dpp v64, v68, v68 row_half_mirror row_mask:0xf bank_mask:0xa\n\t"
    "v_mfma_f32_16x16x32_bf16 v[172:175], a[244:247], a[196:199], v[172:175]\n\t"
    "v_add_f32_dpp v65, v69, v69 row_half_mirror row_mask:0xf bank_mask:0xa\n\t"
    "v_add_f32_dpp v66, v70, v70 row_half_mirror row_mask:0xf bank_mask:0xa\n\t"
    "v_mfma_f32_16x16x32_bf16 v[172:175], a[252:255], a[200:203], v[172:175]\n\t"
    "v_add_f32_dpp v67, v71, v71 row_half_mirror row_mask:0xf bank_mask:0xa\n\t"
    "v_add_f32_dpp v64, v64, v64 quad_perm:[1,0,3,2] row_mask:0xf bank_mask:0xf\n\t"
    "v_mfma_f32_16x16x32_bf16 v[168:171], a[248:251], a[200:203], v[168:171]\n\t"
    "v_add_f32_dpp v65, v65, v65 quad_perm:[1,0,3,2] row_mask:0xf bank_mask:0xf\n\t"
    "v_add_f32_dpp v66, v66, v66 quad_perm:[1,0,3,2] row_mask:0xf bank_mask:0xf\n\t"
    "v_mfma_f32_16x16x32_bf16 v[168:171], a[64:67], a[8:11], v[168:171]\n\t"
    "v_add_f32_dpp v67, v67, v67 quad_perm:[1,0,3,2] row_mask:0xf bank_mask:0xf\n\t"
    "v_add_f32_dpp v64, v64, v64 quad_perm:[2,3,0,1] row_mask:0xf bank_mask:0xf\n\t"
    "v_mfma_f32_16x16x32_bf16 v[172:175], a[68:71], a[8:11], v[172:175]\n\t"
    "v_add_f32_dpp v65, v65, v65 quad_perm:[2,3,0,1] row_mask:0xf bank_mask:0xf\n\t"
    "v_add_f32_dpp v66, v66, v66 quad_perm:[2,3,0,1] row_mask:0xf bank_mask:0xf\n\t"
    "v_mfma_f32_16x16x32_bf16 v[172:175], a[76:79], a[40:43], v[172:175]\n\t"
    "v_add_f32_dpp v67, v67, v67 quad_perm:[2,3,0,1] row_mask:0xf bank_mask:0xf\n\t"
    "ds_write_b128 v36, v[64:67] offset:0\n\t"
    "v_mfma_f32_16x16x32_bf16 v[168:171], a[72:75], a[40:43], v[168:171]\n\t"
    "v_mul_f32 v92, v140, v140\n\t"
    "v_mul_f32 v93, v141, v141\n\t"
    "v_mfma_f32_16x16x32_bf16 v[184:187], a[224:227], a[164:167], v[184:187]\n\t"
    "v_mul_f32 v94, v142, v142\n\t"
    "v_mul_f32 v95, v143, v143\n\t"
    "v_mfma_f32_16x16x32_bf16 v[188:191], a[228:231], a[164:167], v[188:191]\n\t"
    "v_mul_f32 v88, v136, v136\n\t"
    "v_mul_f32 v89, v137, v137\n\t"
    "v_mfma_f32_16x16x32_bf16 v[188:191], a[236:239], a[168:171], v[188:191]\n\t"
    "v_mul_f32 v90, v138, v138\n\t"
    "v_mul_f32 v91, v139, v139\n\t"
    "v_mfma_f32_16x16x32_bf16 v[184:187], a[232:235], a[168:171], v[184:187]\n\t"
    "s_waitcnt lgkmcnt(0)\n\t"
    "s_barrier\n\t"
    "v_mfma_f32_16x16x32_bf16 v[184:187], a[240:243], a[200:203], v[184:187]\n\t"
    "ds_read_b64 v[96:97], v37 offset:0\n\t"
    "ds_read_b64 v[98:99], v37 offset:256\n\t"
    "v_mfma_f32_16x16x32_bf16 v[188:191], a[244:247], a[200:203], v[188:191]\n\t"
    "ds_read_b64 v[100:101], v37 offset:512\n\t"
    "ds_read_b64 v[102:103], v37 offset:768\n\t"
    "v_mfma_f32_16x16x32_bf16 v[188:191], a[252:255], a[204:207], v[188:191]\n\t"
    "v_cndmask_b32 v156, 0, v156, s[28:29]\n\t"
    "v_cndmask_b32 v157, 0, v157, s[28:29]\n\t"
    "v_mfma_f32_16x16x32_bf16 v[184:187], a[248:251], a[204:207], v[184:187]\n\t"
    "v_cndmask_b32 v158, 0, v158, s[28:29]\n\t"
    "v_cndmask_b32 v159, 0, v159, s[28:29]\n\t"
    "v_mfma_f32_16x16x32_bf16 v[184:187], a[64:67], a[12:15], v[184:187]\n\t"
    "v_add_f32 v84, v140, v156\n\t"
    "v_add_f32 v85, v141, v157\n\t"
    "v_mfma_f32_16x16x32_bf16 v[188:191], a[68:71], a[12:15], v[188:191]\n\t"
    "v_add_f32 v86, v142, v158\n\t"
    "v_add_f32 v87, v143, v159\n\t"
    "v_mfma_f32_16x16x32_bf16 v[188:191], a[76:79], a[44:47], v[188:191]\n\t"
    "v_fmac_f32 v92, v156, v156\n\t"
    "v_fmac_f32 v93, v157, v157\n\t"
    "v_mfma_f32_16x16x32_bf16 v[184:187], a[72:75], a[44:47], v[184:187]\n\t"
    "s_waitcnt lgkmcnt(0)\n\t"
    "v_add_f32 v96, v96, v100\n\t"
    "v_mfma_f32_16x16x32_bf16 v[200:203], a[224:227], a[168:171], v[200:203]\n\t"
    "v_add_f32 v97, v97, v101\n\t"
    "v_add_f32 v98, v98, v102\n\t"
    "v_mfma_f32_16x16x32_bf16 v[204:207], a[228:231], a[168:171], v[204:207]\n\t"
    "v_add_f32 v99, v99, v103\n\t"
    "v_min_u32 v96, -2, v96\n\t"
    "v_mfma_f32_16x16x32_bf16 v[204:207], a[236:239], a[172:175], v[204:207]\n\t"
    "v_min_u32 v97, -2, v97\n\t"
    "v_min_u32 v98, -2, v98\n\t"
    "v_mfma_f32_16x16x32_bf16 v[200:203], a[232:235], a[172:175], v[200:203]\n\t"
    "v_min_u32 v99, -2, v99\n\t"
    "buffer_store_dwordx4 v[96:99], v33, s[60:63], 0 offen sc1\n\t"
    "v_mfma_f32_16x16x32_bf16 v[200:203], a[240:243], a[204:207], v[200:203]\n\t"
    "v_cmp_ne_u64 vcc, v[50:51], v[48:49]\n\t"
    "v_fmac_f32 v94, v158, v158\n\t"
    "v_mfma_f32_16x16x32_bf16 v[204:207], a[244:247], a[204:207], v[204:207]\n\t"
    "v_fmac_f32 v95, v159, v159\n\t"
    "v_cndmask_b32 v152, 0, v152, s[28:29]\n\t"
    "v_mfma_f32_16x16x32_bf16 v[204:207], a[252:255], a[208:211], v[204:207]\n\t"
    "v_cndmask_b32 v153, 0, v153, s[28:29]\n\t"
    "v_cndmask_b32 v154, 0, v154, s[28:29]\n\t"
    "v_mfma_f32_16x16x32_bf16 v[200:203], a[248:251], a[208:211], v[200:203]\n\t"
    "v_cndmask_b32 v155, 0, v155, s[28:29]\n\t"
    "v_add_f32 v80, v136, v152\n\t"
    "v_mfma_f32_16x16x32_bf16 v[200:203], a[64:67], a[16:19], v[200:203]\n\t"
    "v_add_f32 v81, v137, v153\n\t"
    "v_add_f32 v82, v138, v154\n\t"
    "v_mfma_f32_16x16x32_bf16 v[204:207], a[68:71], a[16:19], v[204:207]\n\t"
    "v_add_f32 v83, v139, v155\n\t"
    "v_fmac_f32 v88, v152, v152\n\t"
    "v_mfma_f32_16x16x32_bf16 v[204:207], a[76:79], a[48:51], v[204:207]\n\t"
    "v_fmac_f32 v89, v153, v153\n\t"
    "v_fmac_f32 v90, v154, v154\n\t"
    "v_mfma_f32_16x16x32_bf16 v[200:203], a[72:75], a[48:51], v[200:203]\n\t"
    "v_fmac_f32 v91, v155, v155\n\t"
    "v_cndmask_b32 v172, 0, v172, s[30:31]\n\t"
    "v_mfma_f32_16x16x32_bf16 v[216:219], a[224:227], a[172:175], v[216:219]\n\t"
    "v_cndmask_b32 v173, 0, v173, s[30:31]\n\t"
    "v_cndmask_b32 v174, 0, v174, s[30:31]\n\t"
    "v_mfma_f32_16x16x32_bf16 v[220:223], a[228:231], a[172:175], v[220:223]\n\t"
    "v_cndmask_b32 v175, 0, v175, s[30:31]\n\t"
    "v_add_f32 v84, v84, v172\n\t"
    "v_mfma_f32_16x16x32_bf16 v[220:223], a[236:239], a[176:179], v[220:223]\n\t"
    "v_add_f32 v85, v85, v173\n\t"
    "v_add_f32 v86, v86, v174\n\t"
    "v_mfma_f32_16x16x32_bf16 v[216:219], a[232:235], a[176:179], v[216:219]\n\t"
    "v_add_f32 v87, v87, v175\n\t"
    "v_fmac_f32 v92, v172, v172\n\t"
    "v_mfma_f32_16x16x32_bf16 v[216:219], a[240:243], a[208:211], v[216:219]\n\t"
    "v_fmac_f32 v93, v173, v173\n\t"
    "v_fmac_f32 v94, v174, v174\n\t"
    "v_mfma_f32_16x16x32_bf16 v[220:223], a[244:247], a[208:211], v[220:223]\n\t"
    "v_fmac_f32 v95, v175, v175\n\t"
    "v_cndmask_b32 v168, 0, v168, s[30:31]\n\t"
    "v_mfma_f32_16x16x32_bf16 v[220:223], a[252:255], a[212:215], v[220:223]\n\t"
    "v_cndmask_b32 v169, 0, v169, s[30:31]\n\t"
    "v_cndmask_b32 v170, 0, v170, s[30:31]\n\t"
    "v_mfma_f32_16x16x32_bf16 v[216:219], a[248:251], a[212:215], v[216:219]\n\t"
    "v_cndmask_b32 v171, 0, v171, s[30:31]\n\t"
    "v_add_f32 v80, v80, v168\n\t"
    "v_mfma_f32_16x16x32_bf16 v[216:219], a[64:67], a[20:23], v[216:219]\n\t"
    "v_add_f32 v81, v81, v169\n\t"
    "v_add_f32 v82, v82, v170\n\t"
    "v_mfma_f32_16x16x32_bf16 v[220:223], a[68:71], a[20:23], v[220:223]\n\t"
    "v_add_f32 v83, v83, v171\n\t"
    "v_fmac_f32 v88, v168, v168\n\t"
    "v_mfma_f32_16x16x32_bf16 v[220:223], a[76:79], a[52:55], v[220:223]\n\t"
    "v_fmac_f32 v89, v169, v169\n\t"
    "v_fmac_f32 v90, v170, v170\n\t"
    "v_mfma_f32_16x16x32_bf16 v[216:219], a[72:75], a[52:55], v[216:219]\n\t"
    "v_fmac_f32 v91, v171, v171\n\t"
    "v_add_f32 v84, v84, v188\n\t"
    "v_mfma_f32_16x16x32_bf16 v[232:235], a[224:227], a[176:179], v[232:235]\n\t"
    "v_add_f32 v85, v85, v189\n\t"
    "v_add_f32 v86, v86, v190\n\t"
    "v_mfma_f32_16x16x32_bf16 v[236:239], a[228:231], a[176:179], v[236:239]\n\t"
    "v_add_f32 v87, v87, v191\n\t"
    "v_fmac_f32 v92, v188, v188\n\t"
    "v_mfma_f32_16x16x32_bf16 v[236:239], a[236:239], a[180:183], v[236:239]\n\t"
    "v_fmac_f32 v93, v189, v189\n\t"
    "v_fmac_f32 v94, v190, v190\n\t"
    "v_mfma_f32_16x16x32_bf16 v[232:235], a[232:235], a[180:183], v[232:235]\n\t"
    "v_fmac_f32 v95, v191, v191\n\t"
    "v_add_f32 v80, v80, v184\n\t"
    "v_mfma_f32_16x16x32_bf16 v[232:235], a[240:243], a[212:215], v[232:235]\n\t"
    "v_add_f32 v81, v81, v185\n\t"
    "v_add_f32 v82, v82, v186\n\t"
    "v_mfma_f32_16x16x32_bf16 v[236:239], a[244:247], a[212:215], v[236:239]\n\t"
    "v_add_f32 v83, v83, v187\n\t"
    "v_fmac_f32 v88, v184, v184\n\t"
    "v_mfma_f32_16x16x32_bf16 v[236:239], a[252:255], a[216:219], v[236:239]\n\t"
    "v_fmac_f32 v89, v185, v185\n\t"
    "v_fmac_f32 v90, v186, v186\n\t"
    "v_mfma_f32_16x16x32_bf16 v[232:235], a[248:251], a[216:219], v[232:235]\n\t"
    "v_fmac_f32 v91, v187, v187\n\t"
    "v_add_f32 v84, v84, v204\n\t"
    "v_mfma_f32_16x16x32_bf16 v[232:235], a[64:67], a[24:27], v[232:235]\n\t"
    "v_add_f32 v85, v85, v205\n\t"
    "v_add_f32 v86, v86, v206\n\t"
    "v_mfma_f32_16x16x32_bf16 v[236:239], a[68:71], a[24:27], v[236:239]\n\t"
    "v_add_f32 v87, v87, v207\n\t"
    "v_fmac_f32 v92, v204, v204\n\t"
    "v_mfma_f32_16x16x32_bf16 v[236:239], a[76:79], a[56:59], v[236:239]\n\t"
    "v_fmac_f32 v93, v205, v205\n\t"
    "v_fmac_f32 v94, v206, v206\n\t"
    "v_mfma_f32_16x16x32_bf16 v[232:235], a[72:75], a[56:59], v[232:235]\n\t"
    "v_fmac_f32 v95, v207, v207\n\t"
    "v_add_f32 v80, v80, v200\n\t"
    "v_mfma_f32_16x16x32_bf16 v[248:251], a[224:227], a[180:183], v[248:251]\n\t"
    "v_add_f32 v81, v81, v201\n\t"
    "v_add_f32 v82, v82, v202\n\t"
    "v_mfma_f32_16x16x32_bf16 v[252:255], a[228:231], a[180:183], v[252:255]\n\t"
    "v_add_f32 v83, v83, v203\n\t"
    "v_fmac_f32 v88, v200, v200\n\t"
    "v_mfma_f32_16x16x32_bf16 v[252:255], a[236:239], a[184:187], v[252:255]\n\t"
    "v_fmac_f32 v89, v201, v201\n\t"
    "v_fmac_f32 v90, v202, v202\n\t"
    "v_mfma_f32_16x16x32_bf16 v[248:251], a[232:235], a[184:187], v[248:251]\n\t"
    "v_fmac_f32 v91, v203, v203\n\t"
    "v_add_f32 v84, v84, v220\n\t"
    "v_mfma_f32_16x16x32_bf16 v[248:251], a[240:243], a[216:219], v[248:251]\n\t"
    "v_add_f32 v85, v85, v221\n\t"
    "v_add_f32 v86, v86, v222\n\t"
    "v_mfma_f32_16x16x32_bf16 v[252:255], a[244:247], a[216:219], v[252:255]\n\t"
    "v_add_f32 v87, v87, v223\n\t"
    "v_fmac_f32 v92, v220, v220\n\t"
    "v_mfma_f32_16x16x32_bf16 v[252:255], a[252:255], a[220:223], v[252:255]\n\t"
    "v_fmac_f32 v93, v221, v221\n\t"
    "v_fmac_f32 v94, v222, v222\n\t"
    "v_mfma_f32_16x16x32_bf16 v[248:251], a[248:251], a[220:223], v[248:251]\n\t"
    "v_fmac_f32 v95, v223, v223\n\t"
    "v_add_f32 v80, v80, v216\n\t"
    "v_mfma_f32_16x16x32_bf16 v[248:251], a[64:67], a[28:31], v[248:251]\n\t"
    "v_add_f32 v81, v81, v217\n\t"
    "v_add_f32 v82, v82, v218\n\t"
    "v_mfma_f32_16x16x32_bf16 v[252:255], a[68:71], a[28:31], v[252:255]\n\t"
    "v_add_f32 v83, v83, v219\n\t"
    "v_fmac_f32 v88, v216, v216\n\t"
    "v_mfma_f32_16x16x32_bf16 v[252:255], a[76:79], a[60:63], v[252:255]\n\t"
    "v_fmac_f32 v89, v217, v217\n\t"
    "v_fmac_f32 v90, v218, v218\n\t"
    "v_mfma_f32_16x16x32_bf16 v[248:251], a[72:75], a[60:63], v[248:251]\n\t"
    "v_fmac_f32 v91, v219, v219\n\t"
    "s_cbranch_vccnz L_flagwait_%=\n\t"
    "L_flagok_%=:\n\t"
    "s_mov_b32 exec_lo, 0xfff0fff0\n\t"
    "s_mov_b32 exec_hi, 0xfff0fff0\n\t"
    "buffer_load_dwordx4 v[96:99], v38, s[60:63], s8 offen offset:0 sc1\n\t"
    "s_mov_b64 exec, -1\n\t"
    "buffer_load_dwordx4 v[64:67], v38, s[60:63], s9 offen offset:0 sc1\n\t"
    "buffer_load_dwordx4 v[68:71], v38, s[60:63], s10 offen offset:0 sc1\n\t"
    "buffer_load_dwordx4 v[72:75], v38, s[60:63], s11 offen offset:0 sc1\n\t"
    "buffer_load_dwordx4 v[112:115], v38, s[60:63], s12 offen offset:0 sc1\n\t"
    "buffer_load_dwordx4 v[116:119], v38, s[60:63], s13 offen offset:0 sc1\n\t"
    "buffer_load_dwordx4 v[120:123], v38, s[60:63], s14 offen offset:0 sc1\n\t"
    "buffer_load_dwordx4 v[124:127], v38, s[60:63], s15 offen offset:0 sc1\n\t"
    "buffer_store_dwordx2 v[48:49], v32, s[60:63], 0 offen sc1\n\t"
    "v_pk_add_f32 v[84:85], v[84:85], v[236:237]\n\t"
    "v_pk_add_f32 v[86:87], v[86:87], v[238:239]\n\t"
    "v_pk_fma_f32 v[92:93], v[236:237], v[236:237], v[92:93]\n\t"
    "v_pk_fma_f32 v[94:95], v[238:239], v[238:239], v[94:95]\n\t"
    "v_pk_add_f32 v[80:81], v[80:81], v[232:233]\n\t"
    "v_pk_add_f32 v[82:83], v[82:83], v[234:235]\n\t"
    "v_pk_fma_f32 v[88:89], v[232:233], v[232:233], v[88:89]\n\t"
    "v_pk_fma_f32 v[90:91], v[234:235], v[234:235], v[90:91]\n\t"
    "v_pk_add_f32 v[84:85], v[84:85], v[252:253]\n\t"
    "v_pk_add_f32 v[86:87], v[86:87], v[254:255]\n\t"
    "v_pk_fma_f32 v[92:93], v[252:253], v[252:253], v[92:93]\n\t"
    "v_pk_fma_f32 v[94:95], v[254:255], v[254:255], v[94:95]\n\t"
    "v_pk_add_f32 v[80:81], v[80:81], v[248:249]\n\t"
    "v_pk_add_f32 v[82:83], v[82:83], v[250:251]\n\t"
    "v_pk_fma_f32 v[88:89], v[248:249], v[248:249], v[88:89]\n\t"
    "v_pk_fma_f32 v[90:91], v[250:251], v[250:251], v[90:91]\n\t"
    "s_nop 0\n\t"
    "v_add_f32_dpp v80, v80, v80 row_ror:8 row_mask:0xf bank_mask:0x3\n\t"
    "v_add_f32_dpp v81, v81, v81 row_ror:8 row_mask:0xf bank_mask:0x3\n\t"
    "v_add_f32_dpp v82, v82, v82 row_ror:8 row_mask:0xf bank_mask:0x3\n\t"
    "v_add_f32_dpp v83, v83, v83 row_ror:8 row_mask:0xf bank_mask:0x3\n\t"
    "v_add_f32_dpp v84, v84, v84 row_ror:8 row_mask:0xf bank_mask:0x3\n\t"
    "v_add_f32_dpp v85, v85, v85 row_ror:8 row_mask:0xf bank_mask:0x3\n\t"
    "v_add_f32_dpp v86, v86, v86 row_ror:8 row_mask:0xf bank_mask:0x3\n\t"
    "v_add_f32_dpp v87, v87, v87 row_ror:8 row_mask:0xf bank_mask:0x3\n\t"
    "v_add_f32_dpp v80, v88, v88 row_ror:8 row_mask:0xf bank_mask:0xc\n\t"
    "v_add_f32_dpp v81, v89, v89 row_ror:8 row_mask:0xf bank_mask:0xc\n\t"
    "v_add_f32_dpp v82, v90, v90 row_ror:8 row_mask:0xf bank_mask:0xc\n\t"
    "v_add_f32_dpp v83, v91, v91 row_ror:8 row_mask:0xf bank_mask:0xc\n\t"
    "v_add_f32_dpp v84, v92, v92 row_ror:8 row_mask:0xf bank_mask:0xc\n\t"
    "v_add_f32_dpp v85, v93, v93 row_ror:8 row_mask:0xf bank_mask:0xc\n\t"
    "v_add_f32_dpp v86, v94, v94 row_ror:8 row_mask:0xf bank_mask:0xc\n\t"
    "v_add_f32_dpp v87, v95, v95 row_ror:8 row_mask:0xf bank_mask:0xc\n\t"
    "v_add_f32_dpp v80, v80, v80 row_half_mirror row_mask:0xf bank_mask:0x5\n\t"
    "v_add_f32_dpp v81, v81, v81 row_half_mirror row_mask:0xf bank_mask:0x5\n\t"
    "v_add_f32_dpp v82, v82, v82 row_half_mirror row_mask:0xf bank_mask:0x5\n\t"
    "v_add_f32_dpp v83, v83, v83 row_half_mirror row_mask:0xf bank_mask:0x5\n\t"
    "v_add_f32_dpp v80, v84, v84 row_half_mirror row_mask:0xf bank_mask:0xa\n\t"
    "v_add_f32_dpp v81, v85, v85 row_half_mirror row_mask:0xf bank_mask:0xa\n\t"
    "v_add_f32_dpp v82, v86, v86 row_half_mirror row_mask:0xf bank_mask:0xa\n\t"
    "v_add_f32_dpp v83, v87, v87 row_half_mirror row_mask:0xf bank_mask:0xa\n\t"
    "v_add_f32_dpp v80, v80, v80 quad_perm:[1,0,3,2] row_mask:0xf bank_mask:0xf\n\t"
    "v_add_f32_dpp v81, v81, v81 quad_perm:[1,0,3,2] row_mask:0xf bank_mask:0xf\n\t"
    "v_add_f32_dpp v82, v82, v82 quad_perm:[1,0,3,2] row_mask:0xf bank_mask:0xf\n\t"
    "v_add_f32_dpp v83, v83, v83 quad_perm:[1,0,3,2] row_mask:0xf bank_mask:0xf\n\t"
    "v_add_f32_dpp v80, v80, v80 quad_perm:[2,3,0,1] row_mask:0xf bank_mask:0xf\n\t"
    "v_add_f32_dpp v81, v81, v81 quad_perm:[2,3,0,1] row_mask:0xf bank_mask:0xf\n\t"
    "v_add_f32_dpp v82, v82, v82 quad_perm:[2,3,0,1] row_mask:0xf bank_mask:0xf\n\t"
    "v_add_f32_dpp v83, v83, v83 quad_perm:[2,3,0,1] row_mask:0xf bank_mask:0xf\n\t"
    "ds_write_b128 v36, v[80:83] offset:1024\n\t"
    "s_waitcnt lgkmcnt(0)\n\t"
    "s_barrier\n\t"
    "ds_read_b64 v[24:25], v37 offset:1024\n\t"
    "ds_read_b64 v[26:27], v37 offset:1280\n\t"
    "ds_read_b64 v[84:85], v37 offset:1536\n\t"
    "ds_read_b64 v[86:87], v37 offset:1792\n\t"
    "s_waitcnt lgkmcnt(0)\n\t"
    "v_add_f32 v24, v24, v84\n\t"
    "v_add_f32 v25, v25, v85\n\t"
    "v_add_f32 v26, v26, v86\n\t"
    "v_add_f32 v27, v27, v87\n\t"
    "v_min_u32 v24, -2, v24\n\t"
    "v_min_u32 v25, -2, v25\n\t"
    "v_min_u32 v26, -2, v26\n\t"
    "v_min_u32 v27, -2, v27\n\t"
    "buffer_store_dwordx4 v[24:27], v34, s[60:63], 0 offen sc1\n\t"
    "s_mov_b32 s66, 0\n\t"
    "s_mov_b32 s67, 0\n\t"
    "s_mov_b32 s4, 0xc09d30fc\n\t"
    "s_mov_b32 s6, 0x3eeff558\n\t"
    "s_mov_b32 s68, 0x390316a0\n\t"
    "s_waitcnt vmcnt(10)\n\t"
    "v_lshlrev_b32 v104, 16, v57\n\t"
    "v_and_b32 v105, 0xffff0000, v57\n\t"
    "v_lshlrev_b32 v106, 16, v58\n\t"
    "v_and_b32 v107, 0xffff0000, v58\n\t"
    "v_mul_f32 v104, 0x3eeff558, v104\n\t"
    "v_mul_f32 v105, 0x3eeff558, v105\n\t"
    "v_mul_f32 v106, 0x3eeff558, v106\n\t"
    "v_mul_f32 v107, 0x3eeff558, v107\n\t"
    "v_lshlrev_b32 v28, 16, v108\n\t"
    "v_and_b32 v29, 0xffff0000, v108\n\t"
    "v_lshlrev_b32 v30, 16, v109\n\t"
    "v_and_b32 v31, 0xffff0000, v109\n\t"
    "v_mul_f32 v28, 0x3eeff558, v28\n\t"
    "v_mul_f32 v29, 0x3eeff558, v29\n\t"
    "v_mul_f32 v30, 0x3eeff558, v30\n\t"
    "v_mul_f32 v31, 0x3eeff558, v31\n\t"
    "L_gsumA_%=:\n\t"
    "s_waitcnt vmcnt(8)\n\t"
    "v_pk_add_f32 v[48:49], v[96:97], v[64:65]\n\t"
    "v_pk_add_f32 v[50:51], v[98:99], v[66:67]\n\t"
    "s_waitcnt vmcnt(6)\n\t"
    "v_pk_add_f32 v[48:49], v[48:49], v[68:69]\n\t"
    "v_pk_add_f32 v[50:51], v[50:51], v[70:71]\n\t"
    "v_pk_add_f32 v[48:49], v[48:49], v[72:73]\n\t"
    "v_pk_add_f32 v[50:51], v[50:51], v[74:75]\n\t"
    "s_waitcnt vmcnt(4)\n\t"
    "v_pk_add_f32 v[48:49], v[48:49], v[112:113]\n\t"
    "v_pk_add_f32 v[50:51], v[50:51], v[114:115]\n\t"
    "v_pk_add_f32 v[48:49], v[48:49], v[116:117]\n\t"
    "v_pk_add_f32 v[50:51], v[50:51], v[118:119]\n\t"
    "s_waitcnt vmcnt(2)\n\t"
    "v_pk_add_f32 v[48:49], v[48:49], v[120:121]\n\t"
    "v_pk_add_f32 v[50:51], v[50:51], v[122:123]\n\t"
    "v_pk_add_f32 v[48:49], v[48:49], v[124:125]\n\t"
    "v_pk_add_f32 v[50:51], v[50:51], v[126:127]\n\t"
    "v_cmp_u_f32 vcc, v48, v49\n\t"
    "v_cmp_u_f32 s[64:65], v50, v51\n\t"
    "s_or_b64 vcc, vcc, s[64:65]\n\t"
    "s_cbranch_vccnz L_gchkA_%=\n\t"
    "L_gokA_%=:\n\t"
    "v_add_f32_dpp v48, v48, v48 row_ror:4 row_mask:0xf bank_mask:0xf\n\t"
    "v_add_f32_dpp v49, v49, v49 row_ror:4 row_mask:0xf bank_mask:0xf\n\t"
    "v_add_f32_dpp v50, v50, v50 row_ror:4 row_mask:0xf bank_mask:0xf\n\t"
    "v_add_f32_dpp v51, v51, v51 row_ror:4 row_mask:0xf bank_mask:0xf\n\t"
    "v_add_f32_dpp v48, v48, v48 row_ror:8 row_mask:0xf bank_mask:0xf\n\t"
    "v_add_f32_dpp v49, v49, v49 row_ror:8 row_mask:0xf bank_mask:0xf\n\t"
    "v_add_f32_dpp v50, v50, v50 row_ror:8 row_mask:0xf bank_mask:0xf\n\t"
    "v_add_f32_dpp v51, v51, v51 row_ror:8 row_mask:0xf bank_mask:0xf\n\t"
    "v_pk_mul_f32 v[48:49], v[48:49], s[68:69] op_sel_hi:[1,0]\n\t"
    "v_pk_mul_f32 v[50:51], v[50:51], s[68:69] op_sel_hi:[1,0]\n\t"
    "v_pk_fma_f32 v[50:51], v[48:49], v[48:49], v[50:51] neg_lo:[1,0,0] neg_hi:[1,0,0]\n\t"
    "v_max_f32 v50, 0, v50\n\t"
    "v_max_f32 v51, 0, v51\n\t"
    "v_add_f32 v50, 0x3727c5ac, v50\n\t"
    "v_add_f32 v51, 0x3727c5ac, v51\n\t"
    "v_rsq_f32 v50, v50\n\t"
    "v_rsq_f32 v51, v51\n\t"
    "s_nop 0\n\t"
    "v_pk_mul_f32 v[104:105], v[104:105], v[50:51]\n\t"
    "v_pk_fma_f32 v[106:107], v[48:49], v[104:105], v[106:107] neg_lo:[1,0,0] neg_hi:[1,0,0]\n\t"
    "ds_write_b128 v39, v[104:107]\n\t"
    "ds_read_b128 v[96:99], v40 offset:0\n\t"
    "ds_read_b128 v[100:103], v40 offset:16\n\t"
    "ds_read_b128 v[104:107], v40 offset:128\n\t"
    "ds_read_b128 v[108:111], v40 offset:144\n\t"
    "s_mov_b32 s72, 0\n\t"
    "s_mov_b32 s73, 0x10000\n\t"
    "s_mov_b32 s74, 0x1000\n\t"
    "s_mov_b32 s75, 0x11000\n\t"
    "s_waitcnt lgkmcnt(0)\n\t"
    "v_pk_fma_f32 v[128:129], v[128:129], v[96:97], v[98:99]\n\t"
    "v_pk_fma_f32 v[130:131], v[130:131], v[100:101], v[102:103]\n\t"
    "v_pk_fma_f32 v[132:133], v[132:133], v[104:105], v[106:107]\n\t"
    "v_pk_fma_f32 v[134:135], v[134:135], v[108:109], v[110:111]\n\t"
    "v_pk_fma_f32 v[144:145], v[144:145], v[96:97], v[98:99]\n\t"
    "v_pk_fma_f32 v[146:147], v[146:147], v[100:101], v[102:103]\n\t"
    "v_pk_fma_f32 v[148:149], v[148:149], v[104:105], v[106:107]\n\t"
    "v_pk_fma_f32 v[150:151], v[150:151], v[108:109], v[110:111]\n\t"
    "v_pk_fma_f32 v[64:65], v[128:129], v[128:129], s[4:5] op_sel_hi:[1,1,0] neg_lo:[1,0,0] neg_hi:[1,0,0]\n\t"
    "v_pk_fma_f32 v[66:67], v[130:131], v[130:131], s[4:5] op_sel_hi:[1,1,0] neg_lo:[1,0,0] neg_hi:[1,0,0]\n\t"
    "v_pk_fma_f32 v[68:69], v[132:133], v[132:133], s[4:5] op_sel_hi:[1,1,0] neg_lo:[1,0,0] neg_hi:[1,0,0]\n\t"
    "v_pk_fma_f32 v[70:71], v[134:135], v[134:135], s[4:5] op_sel_hi:[1,1,0] neg_lo:[1,0,0] neg_hi:[1,0,0]\n\t"
    "v_pk_fma_f32 v[72:73], v[144:145], v[144:145], s[4:5] op_sel_hi:[1,1,0] neg_lo:[1,0,0] neg_hi:[1,0,0]\n\t"
    "v_pk_fma_f32 v[74:75], v[146:147], v[146:147], s[4:5] op_sel_hi:[1,1,0] neg_lo:[1,0,0] neg_hi:[1,0,0]\n\t"
    "v_pk_fma_f32 v[76:77], v[148:149], v[148:149], s[4:5] op_sel_hi:[1,1,0] neg_lo:[1,0,0] neg_hi:[1,0,0]\n\t"
    "v_pk_fma_f32 v[78:79], v[150:151], v[150:151], s[4:5] op_sel_hi:[1,1,0] neg_lo:[1,0,0] neg_hi:[1,0,0]\n\t"
    "v_pk_mul_f32 v[64:65], v[64:65], v[128:129]\n\t"
    "v_pk_mul_f32 v[66:67], v[66:67], v[130:131]\n\t"
    "v_pk_mul_f32 v[68:69], v[68:69], v[132:133]\n\t"
    "v_pk_mul_f32 v[70:71], v[70:71], v[134:135]\n\t"
    "v_pk_mul_f32 v[72:73], v[72:73], v[144:145]\n\t"
    "v_pk_mul_f32 v[74:75], v[74:75], v[146:147]\n\t"
    "v_pk_mul_f32 v[76:77], v[76:77], v[148:149]\n\t"
    "v_pk_mul_f32 v[78:79], v[78:79], v[150:151]\n\t"
    "v_exp_f32 v64, v64\n\t"
    "v_exp_f32 v65, v65\n\t"
    "v_exp_f32 v66, v66\n\t"
    "v_exp_f32 v67, v67\n\t"
    "v_exp_f32 v68, v68\n\t"
    "v_exp_f32 v69, v69\n\t"
    "v_exp_f32 v70, v70\n\t"
    "v_exp_f32 v71, v71\n\t"
    "v_exp_f32 v72, v72\n\t"
    "v_exp_f32 v73, v73\n\t"
    "v_exp_f32 v74, v74\n\t"
    "v_exp_f32 v75, v75\n\t"
    "v_exp_f32 v76, v76\n\t"
    "v_exp_f32 v77, v77\n\t"
    "v_exp_f32 v78, v78\n\t"
    "v_exp_f32 v79, v79\n\t"
    "v_pk_fma_f32 v[64:65], v[64:65], s[6:7], s[6:7] op_sel_hi:[1,0,0]\n\t"
    "v_pk_fma_f32 v[66:67], v[66:67], s[6:7], s[6:7] op_sel_hi:[1,0,0]\n\t"
    "v_pk_fma_f32 v[68:69], v[68:69], s[6:7], s[6:7] op_sel_hi:[1,0,0]\n\t"
    "v_pk_fma_f32 v[70:71], v[70:71], s[6:7], s[6:7] op_sel_hi:[1,0,0]\n\t"
    "v_pk_fma_f32 v[72:73], v[72:73], s[6:7], s[6:7] op_sel_hi:[1,0,0]\n\t"
    "v_pk_fma_f32 v[74:75], v[74:75], s[6:7], s[6:7] op_sel_hi:[1,0,0]\n\t"
    "v_pk_fma_f32 v[76:77], v[76:77], s[6:7], s[6:7] op_sel_hi:[1,0,0]\n\t"
    "v_pk_fma_f32 v[78:79], v[78:79], s[6:7], s[6:7] op_sel_hi:[1,0,0]\n\t"
    "v_pk_fma_f32 v[160:161], v[160:161], v[96:97], v[98:99]\n\t"
    "v_pk_fma_f32 v[162:163], v[162:163], v[100:101], v[102:103]\n\t"
    "v_pk_fma_f32 v[164:165], v[164:165], v[104:105], v[106:107]\n\t"
    "v_pk_fma_f32 v[166:167], v[166:167], v[108:109], v[110:111]\n\t"
    "v_pk_fma_f32 v[176:177], v[176:177], v[96:97], v[98:99]\n\t"
    "v_pk_fma_f32 v[178:179], v[178:179], v[100:101], v[102:103]\n\t"
    "v_pk_fma_f32 v[180:181], v[180:181], v[104:105], v[106:107]\n\t"
    "v_pk_fma_f32 v[182:183], v[182:183], v[108:109], v[110:111]\n\t"
    "v_rcp_f32 v64, v64\n\t"
    "v_pk_fma_f32 v[80:81], v[160:161], v[160:161], s[4:5] op_sel_hi:[1,1,0] neg_lo:[1,0,0] neg_hi:[1,0,0]\n\t"
    "v_rcp_f32 v65, v65\n\t"
    "v_pk_fma_f32 v[82:83], v[162:163], v[162:163], s[4:5] op_sel_hi:[1,1,0] neg_lo:[1,0,0] neg_hi:[1,0,0]\n\t"
    "v_rcp_f32 v66, v66\n\t"
    "v_pk_fma_f32 v[84:85], v[164:165], v[164:165], s[4:5] op_sel_hi:[1,1,0] neg_lo:[1,0,0] neg_hi:[1,0,0]\n\t"
    "v_rcp_f32 v67, v67\n\t"
    "v_pk_fma_f32 v[86:87], v[166:167], v[166:167], s[4:5] op_sel_hi:[1,1,0] neg_lo:[1,0,0] neg_hi:[1,0,0]\n\t"
    "v_rcp_f32 v68, v68\n\t"
    "v_pk_fma_f32 v[88:89], v[176:177], v[176:177], s[4:5] op_sel_hi:[1,1,0] neg_lo:[1,0,0] neg_hi:[1,0,0]\n\t"
    "v_rcp_f32 v69, v69\n\t"
    "v_pk_fma_f32 v[90:91], v[178:179], v[178:179], s[4:5] op_sel_hi:[1,1,0] neg_lo:[1,0,0] neg_hi:[1,0,0]\n\t"
    "v_rcp_f32 v70, v70\n\t"
    "v_pk_fma_f32 v[92:93], v[180:181], v[180:181], s[4:5] op_sel_hi:[1,1,0] neg_lo:[1,0,0] neg_hi:[1,0,0]\n\t"
    "v_rcp_f32 v71, v71\n\t"
    "v_pk_fma_f32 v[94:95], v[182:183], v[182:183], s[4:5] op_sel_hi:[1,1,0] neg_lo:[1,0,0] neg_hi:[1,0,0]\n\t"
    "v_rcp_f32 v72, v72\n\t"
    "v_pk_mul_f32 v[80:81], v[80:81], v[160:161]\n\t"
    "v_rcp_f32 v73, v73\n\t"
    "v_pk_mul_f32 v[82:83], v[82:83], v[162:163]\n\t"
    "v_rcp_f32 v74, v74\n\t"
    "v_pk_mul_f32 v[84:85], v[84:85], v[164:165]\n\t"
    "v_rcp_f32 v75, v75\n\t"
    "v_pk_mul_f32 v[86:87], v[86:87], v[166:167]\n\t"
    "v_rcp_f32 v76, v76\n\t"
    "v_pk_mul_f32 v[88:89], v[88:89], v[176:177]\n\t"
    "v_rcp_f32 v77, v77\n\t"
    "v_pk_mul_f32 v[90:91], v[90:91], v[178:179]\n\t"
    "v_rcp_f32 v78, v78\n\t"
    "v_pk_mul_f32 v[92:93], v[92:93], v[180:181]\n\t"
    "v_rcp_f32 v79, v79\n\t"
    "v_pk_mul_f32 v[94:95], v[94:95], v[182:183]\n\t"
    "v_exp_f32 v80, v80\n\t"
    "v_pk_mul_f32 v[128:129], v[128:129], v[64:65]\n\t"
    "v_exp_f32 v81, v81\n\t"
    "v_pk_mul_f32 v[130:131], v[130:131], v[66:67]\n\t"
    "v_exp_f32 v82, v82\n\t"
    "v_cvt_pk_bf16_f32 v0, v128, v129\n\t"
    "v_exp_f32 v83, v83\n\t"
    "v_cvt_pk_bf16_f32 v1, v130, v131\n\t"
    "v_exp_f32 v84, v84\n\t"
    "v_pk_mul_f32 v[144:145], v[144:145], v[72:73]\n\t"
    "v_exp_f32 v85, v85\n\t"
    "v_pk_mul_f32 v[146:147], v[146:147], v[74:75]\n\t"
    "v_exp_f32 v86, v86\n\t"
    "v_cvt_pk_bf16_f32 v4, v144, v145\n\t"
    "v_exp_f32 v87, v87\n\t"
    "v_cvt_pk_bf16_f32 v5, v146, v147\n\t"
    "ds_write2st64_b64 v43, v[0:1], v[4:5] offset0:0 offset1:4\n\t"
    "v_exp_f32 v88, v88\n\t"
    "v_pk_mul_f32 v[132:133], v[132:133], v[68:69]\n\t"
    "v_exp_f32 v89, v89\n\t"
    "v_pk_mul_f32 v[134:135], v[134:135], v[70:71]\n\t"
    "v_exp_f32 v90, v90\n\t"
    "v_cvt_pk_bf16_f32 v2, v132, v133\n\t"
    "v_exp_f32 v91, v91\n\t"
    "v_cvt_pk_bf16_f32 v3, v134, v135\n\t"
    "v_exp_f32 v92, v92\n\t"
    "v_pk_mul_f32 v[148:149], v[148:149], v[76:77]\n\t"
    "v_exp_f32 v93, v93\n\t"
    "v_pk_mul_f32 v[150:151], v[150:151], v[78:79]\n\t"
    "v_exp_f32 v94, v94\n\t"
    "v_cvt_pk_bf16_f32 v6, v148, v149\n\t"
    "v_exp_f32 v95, v95\n\t"
    "v_cvt_pk_bf16_f32 v7, v150, v151\n\t"
    "ds_write2st64_b64 v44, v[2:3], v[6:7] offset0:0 offset1:4\n\t"
    "ds_write_b32 v47, v46 offset:0\n\t"
    "v_pk_fma_f32 v[80:81], v[80:81], s[6:7], s[6:7] op_sel_hi:[1,0,0]\n\t"
    "v_pk_fma_f32 v[82:83], v[82:83], s[6:7], s[6:7] op_sel_hi:[1,0,0]\n\t"
    "v_pk_fma_f32 v[84:85], v[84:85], s[6:7], s[6:7] op_sel_hi:[1,0,0]\n\t"
    "v_pk_fma_f32 v[86:87], v[86:87], s[6:7], s[6:7] op_sel_hi:[1,0,0]\n\t"
    "v_pk_fma_f32 v[88:89], v[88:89], s[6:7], s[6:7] op_sel_hi:[1,0,0]\n\t"
    "v_pk_fma_f32 v[90:91], v[90:91], s[6:7], s[6:7] op_sel_hi:[1,0,0]\n\t"
    "v_pk_fma_f32 v[92:93], v[92:93], s[6:7], s[6:7] op_sel_hi:[1,0,0]\n\t"
    "v_pk_fma_f32 v[94:95], v[94:95], s[6:7], s[6:7] op_sel_hi:[1,0,0]\n\t"
    "v_pk_fma_f32 v[192:193], v[192:193], v[96:97], v[98:99]\n\t"
    "v_pk_fma_f32 v[194:195], v[194:195], v[100:101], v[102:103]\n\t"
    "v_pk_fma_f32 v[196:197], v[196:197], v[104:105], v[106:107]\n\t"
    "v_pk_fma_f32 v[198:199], v[198:199], v[108:109], v[110:111]\n\t"
    "v_pk_fma_f32 v[208:209], v[208:209], v[96:97], v[98:99]\n\t"
    "v_pk_fma_f32 v[210:211], v[210:211], v[100:101], v[102:103]\n\t"
    "v_pk_fma_f32 v[212:213], v[212:213], v[104:105], v[106:107]\n\t"
    "v_pk_fma_f32 v[214:215], v[214:215], v[108:109], v[110:111]\n\t"
    "v_rcp_f32 v80, v80\n\t"
    "v_pk_fma_f32 v[64:65], v[192:193], v[192:193], s[4:5] op_sel_hi:[1,1,0] neg_lo:[1,0,0] neg_hi:[1,0,0]\n\t"
    "v_rcp_f32 v81, v81\n\t"
    "v_pk_fma_f32 v[66:67], v[194:195], v[194:195], s[4:5] op_sel_hi:[1,1,0] neg_lo:[1,0,0] neg_hi:[1,0,0]\n\t"
    "ds_read_b32 v33, v45 offset:0\n\t"
    "ds_read_b128 v[16:19], v42 offset:0\n\t"
    "ds_read_b128 v[20:23], v42 offset:1024\n\t"
    "v_rcp_f32 v82, v82\n\t"
    "v_pk_fma_f32 v[68:69], v[196:197], v[196:197], s[4:5] op_sel_hi:[1,1,0] neg_lo:[1,0,0] neg_hi:[1,0,0]\n\t"
    "v_rcp_f32 v83, v83\n\t"
    "v_pk_fma_f32 v[70:71], v[198:199], v[198:199], s[4:5] op_sel_hi:[1,1,0] neg_lo:[1,0,0] neg_hi:[1,0,0]\n\t"
    "v_rcp_f32 v84, v84\n\t"
    "v_pk_fma_f32 v[72:73], v[208:209], v[208:209], s[4:5] op_sel_hi:[1,1,0] neg_lo:[1,0,0] neg_hi:[1,0,0]\n\t"
    "v_rcp_f32 v85, v85\n\t"
    "v_pk_fma_f32 v[74:75], v[210:211], v[210:211], s[4:5] op_sel_hi:[1,1,0] neg_lo:[1,0,0] neg_hi:[1,0,0]\n\t"
    "v_rcp_f32 v86, v86\n\t"
    "v_pk_fma_f32 v[76:77], v[212:213], v[212:213], s[4:5] op_sel_hi:[1,1,0] neg_lo:[1,0,0] neg_hi:[1,0,0]\n\t"
    "v_rcp_f32 v87, v87\n\t"
    "v_pk_fma_f32 v[78:79], v[214:215], v[214:215], s[4:5] op_sel_hi:[1,1,0] neg_lo:[1,0,0] neg_hi:[1,0,0]\n\t"
    "v_rcp_f32 v88, v88\n\t"
    "v_pk_mul_f32 v[64:65], v[64:65], v[192:193]\n\t"
    "v_rcp_f32 v89, v89\n\t"
    "v_pk_mul_f32 v[66:67], v[66:67], v[194:195]\n\t"
    "v_rcp_f32 v90, v90\n\t"
    "v_pk_mul_f32 v[68:69], v[68:69], v[196:197]\n\t"
    "s_waitcnt lgkmcnt(0)\n\t"
    "v_cmp_eq_u32 vcc, 0, v33\n\t"
    "v_rcp_f32 v91, v91\n\t"
    "v_pk_mul_f32 v[70:71], v[70:71], v[198:199]\n\t"
    "v_rcp_f32 v92, v92\n\t"
    "s_cbranch_vccnz L_rb0_%=\n\t"
    "L_rbok0_%=:\n\t"
    "buffer_store_dwordx4 v[16:19], v41, s[84:87], s72 offen offset:0 nt\n\t"
    "v_pk_mul_f32 v[72:73], v[72:73], v[208:209]\n\t"
    "v_rcp_f32 v93, v93\n\t"
    "v_pk_mul_f32 v[74:75], v[74:75], v[210:211]\n\t"
    "v_rcp_f32 v94, v94\n\t"
    "v_pk_mul_f32 v[76:77], v[76:77], v[212:213]\n\t"
    "v_rcp_f32 v95, v95\n\t"
    "v_pk_mul_f32 v[78:79], v[78:79], v[214:215]\n\t"
    "buffer_store_dwordx4 v[20:23], v41, s[84:87], s73 offen offset:0 nt\n\t"
    "s_mov_b32 exec_lo, 0xfff0fff0\n\t"
    "s_mov_b32 exec_hi, 0xfff0fff0\n\t"
    "buffer_load_dwordx4 v[24:27], v38, s[60:63], s8 offen offset:512 sc1\n\t"
    "s_mov_b64 exec, -1\n\t"
    "buffer_load_dwordx4 v[112:115], v38, s[60:63], s9 offen offset:512 sc1\n\t"
    "buffer_load_dwordx4 v[116:119], v38, s[60:63], s10 offen offset:512 sc1\n\t"
    "buffer_load_dwordx4 v[120:123], v38, s[60:63], s11 offen offset:512 sc1\n\t"
    "buffer_load_dwordx4 v[124:127], v38, s[60:63], s12 offen offset:512 sc1\n\t"
    "buffer_load_dwordx4 v[34:37], v38, s[60:63], s13 offen offset:512 sc1\n\t"
    "buffer_load_dwordx4 v[56:59], v38, s[60:63], s14 offen offset:512 sc1\n\t"
    "buffer_load_dwordx4 v[52:55], v38, s[60:63], s15 offen offset:512 sc1\n\t"
    "v_exp_f32 v64, v64\n\t"
    "v_pk_mul_f32 v[160:161], v[160:161], v[80:81]\n\t"
    "v_exp_f32 v65, v65\n\t"
    "v_pk_mul_f32 v[162:163], v[162:163], v[82:83]\n\t"
    "v_exp_f32 v66, v66\n\t"
    "v_cvt_pk_bf16_f32 v8, v160, v161\n\t"
    "v_exp_f32 v67, v67\n\t"
    "v_cvt_pk_bf16_f32 v9, v162, v163\n\t"
    "v_exp_f32 v68, v68\n\t"
    "v_pk_mul_f32 v[176:177], v[176:177], v[88:89]\n\t"
    "v_exp_f32 v69, v69\n\t"
    "v_pk_mul_f32 v[178:179], v[178:179], v[90:91]\n\t"
    "v_exp_f32 v70, v70\n\t"
    "v_cvt_pk_bf16_f32 v12, v176, v177\n\t"
    "v_exp_f32 v71, v71\n\t"
    "v_cvt_pk_bf16_f32 v13, v178, v179\n\t"
    "ds_write2st64_b64 v43, v[8:9], v[12:13] offset0:16 offset1:20\n\t"
    "v_exp_f32 v72, v72\n\t"
    "v_pk_mul_f32 v[164:165], v[164:165], v[84:85]\n\t"
    "v_exp_f32 v73, v73\n\t"
    "v_pk_mul_f32 v[166:167], v[166:167], v[86:87]\n\t"
    "v_exp_f32 v74, v74\n\t"
    "v_cvt_pk_bf16_f32 v10, v164, v165\n\t"
    "v_exp_f32 v75, v75\n\t"
    "v_cvt_pk_bf16_f32 v11, v166, v167\n\t"
    "v_exp_f32 v76, v76\n\t"
    "v_pk_mul_f32 v[180:181], v[180:181], v[92:93]\n\t"
    "v_exp_f32 v77, v77\n\t"
    "v_pk_mul_f32 v[182:183], v[182:183], v[94:95]\n\t"
    "v_exp_f32 v78, v78\n\t"
    "v_cvt_pk_bf16_f32 v14, v180, v181\n\t"
    "v_exp_f32 v79, v79\n\t"
    "v_cvt_pk_bf16_f32 v15, v182, v183\n\t"
    "ds_write2st64_b64 v44, v[10:11], v[14:15] offset0:16 offset1:20\n\t"
    "ds_write_b32 v47, v46 offset:4\n\t"
    "v_pk_fma_f32 v[64:65], v[64:65], s[6:7], s[6:7] op_sel_hi:[1,0,0]\n\t"
    "v_pk_fma_f32 v[66:67], v[66:67], s[6:7], s[6:7] op_sel_hi:[1,0,0]\n\t"
    "v_pk_fma_f32 v[68:69], v[68:69], s[6:7], s[6:7] op_sel_hi:[1,0,0]\n\t"
    "v_pk_fma_f32 v[70:71], v[70:71], s[6:7], s[6:7] op_sel_hi:[1,0,0]\n\t"
    "v_pk_fma_f32 v[72:73], v[72:73], s[6:7], s[6:7] op_sel_hi:[1,0,0]\n\t"
    "v_pk_fma_f32 v[74:75], v[74:75], s[6:7], s[6:7] op_sel_hi:[1,0,0]\n\t"
    "v_pk_fma_f32 v[76:77], v[76:77], s[6:7], s[6:7] op_sel_hi:[1,0,0]\n\t"
    "v_pk_fma_f32 v[78:79], v[78:79], s[6:7], s[6:7] op_sel_hi:[1,0,0]\n\t"
    "v_pk_fma_f32 v[224:225], v[224:225], v[96:97], v[98:99]\n\t"
    "v_pk_fma_f32 v[226:227], v[226:227], v[100:101], v[102:103]\n\t"
    "v_pk_fma_f32 v[228:229], v[228:229], v[104:105], v[106:107]\n\t"
    "v_pk_fma_f32 v[230:231], v[230:231], v[108:109], v[110:111]\n\t"
    "v_pk_fma_f32 v[240:241], v[240:241], v[96:97], v[98:99]\n\t"
    "v_pk_fma_f32 v[242:243], v[242:243], v[100:101], v[102:103]\n\t"
    "v_pk_fma_f32 v[244:245], v[244:245], v[104:105], v[106:107]\n\t"
    "v_pk_fma_f32 v[246:247], v[246:247], v[108:109], v[110:111]\n\t"
    "v_rcp_f32 v64, v64\n\t"
    "v_pk_fma_f32 v[80:81], v[224:225], v[224:225], s[4:5] op_sel_hi:[1,1,0] neg_lo:[1,0,0] neg_hi:[1,0,0]\n\t"
    "v_rcp_f32 v65, v65\n\t"
    "v_pk_fma_f32 v[82:83], v[226:227], v[226:227], s[4:5] op_sel_hi:[1,1,0] neg_lo:[1,0,0] neg_hi:[1,0,0]\n\t"
    "ds_read_b32 v33, v45 offset:4\n\t"
    "ds_read_b128 v[16:19], v42 offset:8192\n\t"
    "ds_read_b128 v[20:23], v42 offset:9216\n\t"
    "v_rcp_f32 v66, v66\n\t"
    "v_pk_fma_f32 v[84:85], v[228:229], v[228:229], s[4:5] op_sel_hi:[1,1,0] neg_lo:[1,0,0] neg_hi:[1,0,0]\n\t"
    "v_rcp_f32 v67, v67\n\t"
    "v_pk_fma_f32 v[86:87], v[230:231], v[230:231], s[4:5] op_sel_hi:[1,1,0] neg_lo:[1,0,0] neg_hi:[1,0,0]\n\t"
    "v_rcp_f32 v68, v68\n\t"
    "v_pk_fma_f32 v[88:89], v[240:241], v[240:241], s[4:5] op_sel_hi:[1,1,0] neg_lo:[1,0,0] neg_hi:[1,0,0]\n\t"
    "v_rcp_f32 v69, v69\n\t"
    "v_pk_fma_f32 v[90:91], v[242:243], v[242:243], s[4:5] op_sel_hi:[1,1,0] neg_lo:[1,0,0] neg_hi:[1,0,0]\n\t"
    "v_rcp_f32 v70, v70\n\t"
    "v_pk_fma_f32 v[92:93], v[244:245], v[244:245], s[4:5] op_sel_hi:[1,1,0] neg_lo:[1,0,0] neg_hi:[1,0,0]\n\t"
    "v_rcp_f32 v71, v71\n\t"
    "v_pk_fma_f32 v[94:95], v[246:247], v[246:247], s[4:5] op_sel_hi:[1,1,0] neg_lo:[1,0,0] neg_hi:[1,0,0]\n\t"
    "v_rcp_f32 v72, v72\n\t"
    "v_pk_mul_f32 v[80:81], v[80:81], v[224:225]\n\t"
    "v_rcp_f32 v73, v73\n\t"
    "v_pk_mul_f32 v[82:83], v[82:83], v[226:227]\n\t"
    "v_rcp_f32 v74, v74\n\t"
    "v_pk_mul_f32 v[84:85], v[84:85], v[228:229]\n\t"
    "s_waitcnt lgkmcnt(0)\n\t"
    "v_cmp_eq_u32 vcc, 0, v33\n\t"
    "v_rcp_f32 v75, v75\n\t"
    "v_pk_mul_f32 v[86:87], v[86:87], v[230:231]\n\t"
    "v_rcp_f32 v76, v76\n\t"
    "s_cbranch_vccnz L_rb1_%=\n\t"
    "L_rbok1_%=:\n\t"
    "buffer_store_dwordx4 v[16:19], v41, s[84:87], s72 offen offset:2048 nt\n\t"
    "v_pk_mul_f32 v[88:89], v[88:89], v[240:241]\n\t"
    "v_rcp_f32 v77, v77\n\t"
    "v_pk_mul_f32 v[90:91], v[90:91], v[242:243]\n\t"
    "v_rcp_f32 v78, v78\n\t"
    "v_pk_mul_f32 v[92:93], v[92:93], v[244:245]\n\t"
    "v_rcp_f32 v79, v79\n\t"
    "v_pk_mul_f32 v[94:95], v[94:95], v[246:247]\n\t"
    "buffer_store_dwordx4 v[20:23], v41, s[84:87], s73 offen offset:2048 nt\n\t"
    "v_exp_f32 v80, v80\n\t"
    "v_pk_mul_f32 v[192:193], v[192:193], v[64:65]\n\t"
    "v_exp_f32 v81, v81\n\t"
    "v_pk_mul_f32 v[194:195], v[194:195], v[66:67]\n\t"
    "v_exp_f32 v82, v82\n\t"
    "v_cvt_pk_bf16_f32 v0, v192, v193\n\t"
    "v_exp_f32 v83, v83\n\t"
    "v_cvt_pk_bf16_f32 v1, v194, v195\n\t"
    "v_exp_f32 v84, v84\n\t"
    "v_pk_mul_f32 v[208:209], v[208:209], v[72:73]\n\t"
    "v_exp_f32 v85, v85\n\t"
    "v_pk_mul_f32 v[210:211], v[210:211], v[74:75]\n\t"
    "v_exp_f32 v86, v86\n\t"
    "v_cvt_pk_bf16_f32 v4, v208, v209\n\t"
    "v_exp_f32 v87, v87\n\t"
    "v_cvt_pk_bf16_f32 v5, v210, v211\n\t"
    "ds_write2st64_b64 v43, v[0:1], v[4:5] offset0:32 offset1:36\n\t"
    "v_exp_f32 v88, v88\n\t"
    "v_pk_mul_f32 v[196:197], v[196:197], v[68:69]\n\t"
    "v_exp_f32 v89, v89\n\t"
    "v_pk_mul_f32 v[198:199], v[198:199], v[70:71]\n\t"
    "v_exp_f32 v90, v90\n\t"
    "v_cvt_pk_bf16_f32 v2, v196, v197\n\t"
    "v_exp_f32 v91, v91\n\t"
    "v_cvt_pk_bf16_f32 v3, v198, v199\n\t"
    "v_exp_f32 v92, v92\n\t"
    "v_pk_mul_f32 v[212:213], v[212:213], v[76:77]\n\t"
    "v_exp_f32 v93, v93\n\t"
    "v_pk_mul_f32 v[214:215], v[214:215], v[78:79]\n\t"
    "v_exp_f32 v94, v94\n\t"
    "v_cvt_pk_bf16_f32 v6, v212, v213\n\t"
    "v_exp_f32 v95, v95\n\t"
    "v_cvt_pk_bf16_f32 v7, v214, v215\n\t"
    "ds_write2st64_b64 v44, v[2:3], v[6:7] offset0:32 offset1:36\n\t"
    "ds_write_b32 v47, v46 offset:8\n\t"
    "v_pk_fma_f32 v[80:81], v[80:81], s[6:7], s[6:7] op_sel_hi:[1,0,0]\n\t"
    "v_pk_fma_f32 v[82:83], v[82:83], s[6:7], s[6:7] op_sel_hi:[1,0,0]\n\t"
    "v_pk_fma_f32 v[84:85], v[84:85], s[6:7], s[6:7] op_sel_hi:[1,0,0]\n\t"
    "v_pk_fma_f32 v[86:87], v[86:87], s[6:7], s[6:7] op_sel_hi:[1,0,0]\n\t"
    "v_pk_fma_f32 v[88:89], v[88:89], s[6:7], s[6:7] op_sel_hi:[1,0,0]\n\t"
    "v_pk_fma_f32 v[90:91], v[90:91], s[6:7], s[6:7] op_sel_hi:[1,0,0]\n\t"
    "v_pk_fma_f32 v[92:93], v[92:93], s[6:7], s[6:7] op_sel_hi:[1,0,0]\n\t"
    "v_pk_fma_f32 v[94:95], v[94:95], s[6:7], s[6:7] op_sel_hi:[1,0,0]\n\t"
    "L_gsumB_%=:\n\t"
    "s_waitcnt vmcnt(8)\n\t"
    "v_pk_add_f32 v[48:49], v[24:25], v[112:113]\n\t"
    "v_pk_add_f32 v[50:51], v[26:27], v[114:115]\n\t"
    "s_waitcnt vmcnt(6)\n\t"
    "v_pk_add_f32 v[48:49], v[48:49], v[116:117]\n\t"
    "v_pk_add_f32 v[50:51], v[50:51], v[118:119]\n\t"
    "v_pk_add_f32 v[48:49], v[48:49], v[120:121]\n\t"
    "v_pk_add_f32 v[50:51], v[50:51], v[122:123]\n\t"
    "s_waitcnt vmcnt(4)\n\t"
    "v_pk_add_f32 v[48:49], v[48:49], v[124:125]\n\t"
    "v_pk_add_f32 v[50:51], v[50:51], v[126:127]\n\t"
    "v_pk_add_f32 v[48:49], v[48:49], v[34:35]\n\t"
    "v_pk_add_f32 v[50:51], v[50:51], v[36:37]\n\t"
    "s_waitcnt vmcnt(2)\n\t"
    "v_pk_add_f32 v[48:49], v[48:49], v[56:57]\n\t"
    "v_pk_add_f32 v[50:51], v[50:51], v[58:59]\n\t"
    "v_pk_add_f32 v[48:49], v[48:49], v[52:53]\n\t"
    "v_pk_add_f32 v[50:51], v[50:51], v[54:55]\n\t"
    "v_cmp_u_f32 vcc, v48, v49\n\t"
    "v_cmp_u_f32 s[64:65], v50, v51\n\t"
    "s_or_b64 vcc, vcc, s[64:65]\n\t"
    "s_cbranch_vccnz L_gchkB_%=\n\t"
    "L_gokB_%=:\n\t"
    "v_add_f32_dpp v48, v48, v48 row_ror:4 row_mask:0xf bank_mask:0xf\n\t"
    "v_add_f32_dpp v49, v49, v49 row_ror:4 row_mask:0xf bank_mask:0xf\n\t"
    "v_add_f32_dpp v50, v50, v50 row_ror:4 row_mask:0xf bank_mask:0xf\n\t"
    "v_add_f32_dpp v51, v51, v51 row_ror:4 row_mask:0xf bank_mask:0xf\n\t"
    "v_add_f32_dpp v48, v48, v48 row_ror:8 row_mask:0xf bank_mask:0xf\n\t"
    "v_add_f32_dpp v49, v49, v49 row_ror:8 row_mask:0xf bank_mask:0xf\n\t"
    "v_add_f32_dpp v50, v50, v50 row_ror:8 row_mask:0xf bank_mask:0xf\n\t"
    "v_add_f32_dpp v51, v51, v51 row_ror:8 row_mask:0xf bank_mask:0xf\n\t"
    "v_pk_mul_f32 v[48:49], v[48:49], s[68:69] op_sel_hi:[1,0]\n\t"
    "v_pk_mul_f32 v[50:51], v[50:51], s[68:69] op_sel_hi:[1,0]\n\t"
    "v_pk_fma_f32 v[50:51], v[48:49], v[48:49], v[50:51] neg_lo:[1,0,0] neg_hi:[1,0,0]\n\t"
    "v_max_f32 v50, 0, v50\n\t"
    "v_max_f32 v51, 0, v51\n\t"
    "v_add_f32 v50, 0x3727c5ac, v50\n\t"
    "v_add_f32 v51, 0x3727c5ac, v51\n\t"
    "v_rsq_f32 v50, v50\n\t"
    "v_rsq_f32 v51, v51\n\t"
    "s_nop 0\n\t"
    "v_pk_mul_f32 v[28:29], v[28:29], v[50:51]\n\t"
    "v_pk_fma_f32 v[30:31], v[48:49], v[28:29], v[30:31] neg_lo:[1,0,0] neg_hi:[1,0,0]\n\t"
    "ds_write_b128 v39, v[28:31]\n\t"
    "ds_read_b128 v[112:115], v40 offset:0\n\t"
    "ds_read_b128 v[116:119], v40 offset:16\n\t"
    "ds_read_b128 v[120:123], v40 offset:128\n\t"
    "ds_read_b128 v[124:127], v40 offset:144\n\t"
    "ds_read_b32 v33, v45 offset:8\n\t"
    "ds_read_b128 v[16:19], v42 offset:16384\n\t"
    "ds_read_b128 v[20:23], v42 offset:17408\n\t"
    "v_rcp_f32 v80, v80\n\t"
    "v_rcp_f32 v81, v81\n\t"
    "v_rcp_f32 v82, v82\n\t"
    "v_rcp_f32 v83, v83\n\t"
    "v_rcp_f32 v84, v84\n\t"
    "v_rcp_f32 v85, v85\n\t"
    "v_rcp_f32 v86, v86\n\t"
    "v_rcp_f32 v87, v87\n\t"
    "s_waitcnt lgkmcnt(0)\n\t"
    "v_cmp_eq_u32 vcc, 0, v33\n\t"
    "v_rcp_f32 v88, v88\n\t"
    "v_rcp_f32 v89, v89\n\t"
    "s_cbranch_vccnz L_rb2_%=\n\t"
    "L_rbok2_%=:\n\t"
    "v_pk_fma_f32 v[136:137], v[136:137], v[112:113], v[114:115]\n\t"
    "v_pk_fma_f32 v[138:139], v[138:139], v[116:117], v[118:119]\n\t"
    "v_pk_fma_f32 v[140:141], v[140:141], v[120:121], v[122:123]\n\t"
    "v_pk_fma_f32 v[142:143], v[142:143], v[124:125], v[126:127]\n\t"
    "v_pk_fma_f32 v[152:153], v[152:153], v[112:113], v[114:115]\n\t"
    "v_pk_fma_f32 v[154:155], v[154:155], v[116:117], v[118:119]\n\t"
    "buffer_store_dwordx4 v[16:19], v41, s[84:87], s74 offen offset:0 nt\n\t"
    "v_pk_fma_f32 v[156:157], v[156:157], v[120:121], v[122:123]\n\t"
    "v_pk_fma_f32 v[158:159], v[158:159], v[124:125], v[126:127]\n\t"
    "v_pk_fma_f32 v[64:65], v[136:137], v[136:137], s[4:5] op_sel_hi:[1,1,0] neg_lo:[1,0,0] neg_hi:[1,0,0]\n\t"
    "v_pk_fma_f32 v[66:67], v[138:139], v[138:139], s[4:5] op_sel_hi:[1,1,0] neg_lo:[1,0,0] neg_hi:[1,0,0]\n\t"
    "v_pk_fma_f32 v[68:69], v[140:141], v[140:141], s[4:5] op_sel_hi:[1,1,0] neg_lo:[1,0,0] neg_hi:[1,0,0]\n\t"
    "v_pk_fma_f32 v[70:71], v[142:143], v[142:143], s[4:5] op_sel_hi:[1,1,0] neg_lo:[1,0,0] neg_hi:[1,0,0]\n\t"
    "v_pk_fma_f32 v[72:73], v[152:153], v[152:153], s[4:5] op_sel_hi:[1,1,0] neg_lo:[1,0,0] neg_hi:[1,0,0]\n\t"
    "v_pk_fma_f32 v[74:75], v[154:155], v[154:155], s[4:5] op_sel_hi:[1,1,0] neg_lo:[1,0,0] neg_hi:[1,0,0]\n\t"
    "v_pk_fma_f32 v[76:77], v[156:157], v[156:157], s[4:5] op_sel_hi:[1,1,0] neg_lo:[1,0,0] neg_hi:[1,0,0]\n\t"
    "v_pk_fma_f32 v[78:79], v[158:159], v[158:159], s[4:5] op_sel_hi:[1,1,0] neg_lo:[1,0,0] neg_hi:[1,0,0]\n\t"
    "buffer_store_dwordx4 v[20:23], v41, s[84:87], s75 offen offset:0 nt\n\t"
    "v_pk_mul_f32 v[64:65], v[64:65], v[136:137]\n\t"
    "v_pk_mul_f32 v[66:67], v[66:67], v[138:139]\n\t"
    "v_rcp_f32 v90, v90\n\t"
    "v_pk_mul_f32 v[68:69], v[68:69], v[140:141]\n\t"
    "v_rcp_f32 v91, v91\n\t"
    "v_pk_mul_f32 v[70:71], v[70:71], v[142:143]\n\t"
    "v_rcp_f32 v92, v92\n\t"
    "v_pk_mul_f32 v[72:73], v[72:73], v[152:153]\n\t"
    "v_rcp_f32 v93, v93\n\t"
    "v_pk_mul_f32 v[74:75], v[74:75], v[154:155]\n\t"
    "v_rcp_f32 v94, v94\n\t"
    "v_pk_mul_f32 v[76:77], v[76:77], v[156:157]\n\t"
    "v_rcp_f32 v95, v95\n\t"
    "v_pk_mul_f32 v[78:79], v[78:79], v[158:159]\n\t"
    "v_exp_f32 v64, v64\n\t"
    "v_pk_mul_f32 v[224:225], v[224:225], v[80:81]\n\t"
    "v_exp_f32 v65, v65\n\t"
    "v_pk_mul_f32 v[226:227], v[226:227], v[82:83]\n\t"
    "v_exp_f32 v66, v66\n\t"
    "v_cvt_pk_bf16_f32 v8, v224, v225\n\t"
    "v_exp_f32 v67, v67\n\t"
    "v_cvt_pk_bf16_f32 v9, v226, v227\n\t"
    "v_exp_f32 v68, v68\n\t"
    "v_pk_mul_f32 v[240:241], v[240:241], v[88:89]\n\t"
    "v_exp_f32 v69, v69\n\t"
    "v_pk_mul_f32 v[242:243], v[242:243], v[90:91]\n\t"
    "v_exp_f32 v70, v70\n\t"
    "v_cvt_pk_bf16_f32 v12, v240, v241\n\t"
    "v_exp_f32 v71, v71\n\t"
    "v_cvt_pk_bf16_f32 v13, v242, v243\n\t"
    "ds_write2st64_b64 v43, v[8:9], v[12:13] offset0:48 offset1:52\n\t"
    "v_exp_f32 v72, v72\n\t"
    "v_pk_mul_f32 v[228:229], v[228:229], v[84:85]\n\t"
    "v_exp_f32 v73, v73\n\t"
    "v_pk_mul_f32 v[230:231], v[230:231], v[86:87]\n\t"
    "v_exp_f32 v74, v74\n\t"
    "v_cvt_pk_bf16_f32 v10, v228, v229\n\t"
    "v_exp_f32 v75, v75\n\t"
    "v_cvt_pk_bf16_f32 v11, v230, v231\n\t"
    "v_exp_f32 v76, v76\n\t"
    "v_pk_mul_f32 v[244:245], v[244:245], v[92:93]\n\t"
    "v_exp_f32 v77, v77\n\t"
    "v_pk_mul_f32 v[246:247], v[246:247], v[94:95]\n\t"
    "v_exp_f32 v78, v78\n\t"
    "v_cvt_pk_bf16_f32 v14, v244, v245\n\t"
    "v_exp_f32 v79, v79\n\t"
    "v_cvt_pk_bf16_f32 v15, v246, v247\n\t"
    "ds_write2st64_b64 v44, v[10:11], v[14:15] offset0:48 offset1:52\n\t"
    "ds_write_b32 v47, v46 offset:12\n\t"
    "v_pk_fma_f32 v[64:65], v[64:65], s[6:7], s[6:7] op_sel_hi:[1,0,0]\n\t"
    "v_pk_fma_f32 v[66:67], v[66:67], s[6:7], s[6:7] op_sel_hi:[1,0,0]\n\t"
    "v_pk_fma_f32 v[68:69], v[68:69], s[6:7], s[6:7] op_sel_hi:[1,0,0]\n\t"
    "v_pk_fma_f32 v[70:71], v[70:71], s[6:7], s[6:7] op_sel_hi:[1,0,0]\n\t"
    "v_pk_fma_f32 v[72:73], v[72:73], s[6:7], s[6:7] op_sel_hi:[1,0,0]\n\t"
    "v_pk_fma_f32 v[74:75], v[74:75], s[6:7], s[6:7] op_sel_hi:[1,0,0]\n\t"
    "v_pk_fma_f32 v[76:77], v[76:77], s[6:7], s[6:7] op_sel_hi:[1,0,0]\n\t"
    "v_pk_fma_f32 v[78:79], v[78:79], s[6:7], s[6:7] op_sel_hi:[1,0,0]\n\t"
    "v_pk_fma_f32 v[168:169], v[168:169], v[112:113], v[114:115]\n\t"
    "v_pk_fma_f32 v[170:171], v[170:171], v[116:117], v[118:119]\n\t"
    "v_pk_fma_f32 v[172:173], v[172:173], v[120:121], v[122:123]\n\t"
    "v_pk_fma_f32 v[174:175], v[174:175], v[124:125], v[126:127]\n\t"
    "v_pk_fma_f32 v[184:185], v[184:185], v[112:113], v[114:115]\n\t"
    "v_pk_fma_f32 v[186:187], v[186:187], v[116:117], v[118:119]\n\t"
    "v_pk_fma_f32 v[188:189], v[188:189], v[120:121], v[122:123]\n\t"
    "v_pk_fma_f32 v[190:191], v[190:191], v[124:125], v[126:127]\n\t"
    "v_rcp_f32 v64, v64\n\t"
    "v_pk_fma_f32 v[80:81], v[168:169], v[168:169], s[4:5] op_sel_hi:[1,1,0] neg_lo:[1,0,0] neg_hi:[1,0,0]\n\t"
    "v_rcp_f32 v65, v65\n\t"
    "v_pk_fma_f32 v[82:83], v[170:171], v[170:171], s[4:5] op_sel_hi:[1,1,0] neg_lo:[1,0,0] neg_hi:[1,0,0]\n\t"
    "ds_read_b32 v33, v45 offset:12\n\t"
    "ds_read_b128 v[16:19], v42 offset:24576\n\t"
    "ds_read_b128 v[20:23], v42 offset:25600\n\t"
    "v_rcp_f32 v66, v66\n\t"
    "v_pk_fma_f32 v[84:85], v[172:173], v[172:173], s[4:5] op_sel_hi:[1,1,0] neg_lo:[1,0,0] neg_hi:[1,0,0]\n\t"
    "v_rcp_f32 v67, v67\n\t"
    "v_pk_fma_f32 v[86:87], v[174:175], v[174:175], s[4:5] op_sel_hi:[1,1,0] neg_lo:[1,0,0] neg_hi:[1,0,0]\n\t"
    "v_rcp_f32 v68, v68\n\t"
    "v_pk_fma_f32 v[88:89], v[184:185], v[184:185], s[4:5] op_sel_hi:[1,1,0] neg_lo:[1,0,0] neg_hi:[1,0,0]\n\t"
    "v_rcp_f32 v69, v69\n\t"
    "v_pk_fma_f32 v[90:91], v[186:187], v[186:187], s[4:5] op_sel_hi:[1,1,0] neg_lo:[1,0,0] neg_hi:[1,0,0]\n\t"
    "v_rcp_f32 v70, v70\n\t"
    "v_pk_fma_f32 v[92:93], v[188:189], v[188:189], s[4:5] op_sel_hi:[1,1,0] neg_lo:[1,0,0] neg_hi:[1,0,0]\n\t"
    "v_rcp_f32 v71, v71\n\t"
    "v_pk_fma_f32 v[94:95], v[190:191], v[190:191], s[4:5] op_sel_hi:[1,1,0] neg_lo:[1,0,0] neg_hi:[1,0,0]\n\t"
    "v_rcp_f32 v72, v72\n\t"
    "v_pk_mul_f32 v[80:81], v[80:81], v[168:169]\n\t"
    "v_rcp_f32 v73, v73\n\t"
    "v_pk_mul_f32 v[82:83], v[82:83], v[170:171]\n\t"
    "v_rcp_f32 v74, v74\n\t"
    "v_pk_mul_f32 v[84:85], v[84:85], v[172:173]\n\t"
    "s_waitcnt lgkmcnt(0)\n\t"
    "v_cmp_eq_u32 vcc, 0, v33\n\t"
    "v_rcp_f32 v75, v75\n\t"
    "v_pk_mul_f32 v[86:87], v[86:87], v[174:175]\n\t"
    "v_rcp_f32 v76, v76\n\t"
    "s_cbranch_vccnz L_rb3_%=\n\t"
    "L_rbok3_%=:\n\t"
    "buffer_store_dwordx4 v[16:19], v41, s[84:87], s74 offen offset:2048 nt\n\t"
    "v_pk_mul_f32 v[88:89], v[88:89], v[184:185]\n\t"
    "v_rcp_f32 v77, v77\n\t"
    "v_pk_mul_f32 v[90:91], v[90:91], v[186:187]\n\t"
    "v_rcp_f32 v78, v78\n\t"
    "v_pk_mul_f32 v[92:93], v[92:93], v[188:189]\n\t"
    "v_rcp_f32 v79, v79\n\t"
    "v_pk_mul_f32 v[94:95], v[94:95], v[190:191]\n\t"
    "buffer_store_dwordx4 v[20:23], v41, s[84:87], s75 offen offset:2048 nt\n\t"
    "v_exp_f32 v80, v80\n\t"
    "v_pk_mul_f32 v[136:137], v[136:137], v[64:65]\n\t"
    "v_exp_f32 v81, v81\n\t"
    "v_pk_mul_f32 v[138:139], v[138:139], v[66:67]\n\t"
    "v_exp_f32 v82, v82\n\t"
    "v_cvt_pk_bf16_f32 v0, v136, v137\n\t"
    "v_exp_f32 v83, v83\n\t"
    "v_cvt_pk_bf16_f32 v1, v138, v139\n\t"
    "v_exp_f32 v84, v84\n\t"
    "v_pk_mul_f32 v[152:153], v[152:153], v[72:73]\n\t"
    "v_exp_f32 v85, v85\n\t"
    "v_pk_mul_f32 v[154:155], v[154:155], v[74:75]\n\t"
    "v_exp_f32 v86, v86\n\t"
    "v_cvt_pk_bf16_f32 v4, v152, v153\n\t"
    "v_exp_f32 v87, v87\n\t"
    "v_cvt_pk_bf16_f32 v5, v154, v155\n\t"
    "ds_write2st64_b64 v43, v[0:1], v[4:5] offset0:64 offset1:68\n\t"
    "v_exp_f32 v88, v88\n\t"
    "v_pk_mul_f32 v[140:141], v[140:141], v[68:69]\n\t"
    "v_exp_f32 v89, v89\n\t"
    "v_pk_mul_f32 v[142:143], v[142:143], v[70:71]\n\t"
    "v_exp_f32 v90, v90\n\t"
    "v_cvt_pk_bf16_f32 v2, v140, v141\n\t"
    "v_exp_f32 v91, v91\n\t"
    "v_cvt_pk_bf16_f32 v3, v142, v143\n\t"
    "v_exp_f32 v92, v92\n\t"
    "v_pk_mul_f32 v[156:157], v[156:157], v[76:77]\n\t"
    "v_exp_f32 v93, v93\n\t"
    "v_pk_mul_f32 v[158:159], v[158:159], v[78:79]\n\t"
    "v_exp_f32 v94, v94\n\t"
    "v_cvt_pk_bf16_f32 v6, v156, v157\n\t"
    "v_exp_f32 v95, v95\n\t"
    "v_cvt_pk_bf16_f32 v7, v158, v159\n\t"
    "ds_write2st64_b64 v44, v[2:3], v[6:7] offset0:64 offset1:68\n\t"
    "ds_write_b32 v47, v46 offset:16\n\t"
    "v_pk_fma_f32 v[80:81], v[80:81], s[6:7], s[6:7] op_sel_hi:[1,0,0]\n\t"
    "v_pk_fma_f32 v[82:83], v[82:83], s[6:7], s[6:7] op_sel_hi:[1,0,0]\n\t"
    "v_pk_fma_f32 v[84:85], v[84:85], s[6:7], s[6:7] op_sel_hi:[1,0,0]\n\t"
    "v_pk_fma_f32 v[86:87], v[86:87], s[6:7], s[6:7] op_sel_hi:[1,0,0]\n\t"
    "v_pk_fma_f32 v[88:89], v[88:89], s[6:7], s[6:7] op_sel_hi:[1,0,0]\n\t"
    "v_pk_fma_f32 v[90:91], v[90:91], s[6:7], s[6:7] op_sel_hi:[1,0,0]\n\t"
    "v_pk_fma_f32 v[92:93], v[92:93], s[6:7], s[6:7] op_sel_hi:[1,0,0]\n\t"
    "v_pk_fma_f32 v[94:95], v[94:95], s[6:7], s[6:7] op_sel_hi:[1,0,0]\n\t"
    "v_pk_fma_f32 v[200:201], v[200:201], v[112:113], v[114:115]\n\t"
    "v_pk_fma_f32 v[202:203], v[202:203], v[116:117], v[118:119]\n\t"
    "v_pk_fma_f32 v[204:205], v[204:205], v[120:121], v[122:123]\n\t"
    "v_pk_fma_f32 v[206:207], v[206:207], v[124:125], v[126:127]\n\t"
    "v_pk_fma_f32 v[216:217], v[216:217], v[112:113], v[114:115]\n\t"
    "v_pk_fma_f32 v[218:219], v[218:219], v[116:117], v[118:119]\n\t"
    "v_pk_fma_f32 v[220:221], v[220:221], v[120:121], v[122:123]\n\t"
    "v_pk_fma_f32 v[222:223], v[222:223], v[124:125], v[126:127]\n\t"
    "v_rcp_f32 v80, v80\n\t"
    "v_pk_fma_f32 v[64:65], v[200:201], v[200:201], s[4:5] op_sel_hi:[1,1,0] neg_lo:[1,0,0] neg_hi:[1,0,0]\n\t"
    "v_rcp_f32 v81, v81\n\t"
    "v_pk_fma_f32 v[66:67], v[202:203], v[202:203], s[4:5] op_sel_hi:[1,1,0] neg_lo:[1,0,0] neg_hi:[1,0,0]\n\t"
    "ds_read_b32 v33, v45 offset:16\n\t"
    "ds_read_b128 v[16:19], v42 offset:32768\n\t"
    "ds_read_b128 v[20:23], v42 offset:33792\n\t"
    "v_rcp_f32 v82, v82\n\t"
    "v_pk_fma_f32 v[68:69], v[204:205], v[204:205], s[4:5] op_sel_hi:[1,1,0] neg_lo:[1,0,0] neg_hi:[1,0,0]\n\t"
    "v_rcp_f32 v83, v83\n\t"
    "v_pk_fma_f32 v[70:71], v[206:207], v[206:207], s[4:5] op_sel_hi:[1,1,0] neg_lo:[1,0,0] neg_hi:[1,0,0]\n\t"
    "v_rcp_f32 v84, v84\n\t"
    "v_pk_fma_f32 v[72:73], v[216:217], v[216:217], s[4:5] op_sel_hi:[1,1,0] neg_lo:[1,0,0] neg_hi:[1,0,0]\n\t"
    "v_rcp_f32 v85, v85\n\t"
    "v_pk_fma_f32 v[74:75], v[218:219], v[218:219], s[4:5] op_sel_hi:[1,1,0] neg_lo:[1,0,0] neg_hi:[1,0,0]\n\t"
    "v_rcp_f32 v86, v86\n\t"
    "v_pk_fma_f32 v[76:77], v[220:221], v[220:221], s[4:5] op_sel_hi:[1,1,0] neg_lo:[1,0,0] neg_hi:[1,0,0]\n\t"
    "v_rcp_f32 v87, v87\n\t"
    "v_pk_fma_f32 v[78:79], v[222:223], v[222:223], s[4:5] op_sel_hi:[1,1,0] neg_lo:[1,0,0] neg_hi:[1,0,0]\n\t"
    "v_rcp_f32 v88, v88\n\t"
    "v_pk_mul_f32 v[64:65], v[64:65], v[200:201]\n\t"
    "v_rcp_f32 v89, v89\n\t"
    "v_pk_mul_f32 v[66:67], v[66:67], v[202:203]\n\t"
    "v_rcp_f32 v90, v90\n\t"
    "v_pk_mul_f32 v[68:69], v[68:69], v[204:205]\n\t"
    "s_waitcnt lgkmcnt(0)\n\t"
    "v_cmp_eq_u32 vcc, 0, v33\n\t"
    "v_rcp_f32 v91, v91\n\t"
    "v_pk_mul_f32 v[70:71], v[70:71], v[206:207]\n\t"
    "v_rcp_f32 v92, v92\n\t"
    "s_cbranch_vccnz L_rb4_%=\n\t"
    "L_rbok4_%=:\n\t"
    "buffer_store_dwordx4 v[16:19], v41, s[84:87], s72 offen offset:128 nt\n\t"
    "v_pk_mul_f32 v[72:73], v[72:73], v[216:217]\n\t"
    "v_rcp_f32 v93, v93\n\t"
    "v_pk_mul_f32 v[74:75], v[74:75], v[218:219]\n\t"
    "v_rcp_f32 v94, v94\n\t"
    "v_pk_mul_f32 v[76:77], v[76:77], v[220:221]\n\t"
    "v_rcp_f32 v95, v95\n\t"
    "v_pk_mul_f32 v[78:79], v[78:79], v[222:223]\n\t"
    "buffer_store_dwordx4 v[20:23], v41, s[84:87], s73 offen offset:128 nt\n\t"
    "v_exp_f32 v64, v64\n\t"
    "v_pk_mul_f32 v[168:169], v[168:169], v[80:81]\n\t"
    "v_exp_f32 v65, v65\n\t"
    "v_pk_mul_f32 v[170:171], v[170:171], v[82:83]\n\t"
    "v_exp_f32 v66, v66\n\t"
    "v_cvt_pk_bf16_f32 v8, v168, v169\n\t"
    "v_exp_f32 v67, v67\n\t"
    "v_cvt_pk_bf16_f32 v9, v170, v171\n\t"
    "v_exp_f32 v68, v68\n\t"
    "v_pk_mul_f32 v[184:185], v[184:185], v[88:89]\n\t"
    "v_exp_f32 v69, v69\n\t"
    "v_pk_mul_f32 v[186:187], v[186:187], v[90:91]\n\t"
    "v_exp_f32 v70, v70\n\t"
    "v_cvt_pk_bf16_f32 v12, v184, v185\n\t"
    "v_exp_f32 v71, v71\n\t"
    "v_cvt_pk_bf16_f32 v13, v186, v187\n\t"
    "ds_write2st64_b64 v43, v[8:9], v[12:13] offset0:80 offset1:84\n\t"
    "v_exp_f32 v72, v72\n\t"
    "v_pk_mul_f32 v[172:173], v[172:173], v[84:85]\n\t"
    "v_exp_f32 v73, v73\n\t"
    "v_pk_mul_f32 v[174:175], v[174:175], v[86:87]\n\t"
    "v_exp_f32 v74, v74\n\t"
    "v_cvt_pk_bf16_f32 v10, v172, v173\n\t"
    "v_exp_f32 v75, v75\n\t"
    "v_cvt_pk_bf16_f32 v11, v174, v175\n\t"
    "v_exp_f32 v76, v76\n\t"
    "v_pk_mul_f32 v[188:189], v[188:189], v[92:93]\n\t"
    "v_exp_f32 v77, v77\n\t"
    "v_pk_mul_f32 v[190:191], v[190:191], v[94:95]\n\t"
    "v_exp_f32 v78, v78\n\t"
    "v_cvt_pk_bf16_f32 v14, v188, v189\n\t"
    "v_exp_f32 v79, v79\n\t"
    "v_cvt_pk_bf16_f32 v15, v190, v191\n\t"
    "ds_write2st64_b64 v44, v[10:11], v[14:15] offset0:80 offset1:84\n\t"
    "ds_write_b32 v47, v46 offset:20\n\t"
    "v_pk_fma_f32 v[64:65], v[64:65], s[6:7], s[6:7] op_sel_hi:[1,0,0]\n\t"
    "v_pk_fma_f32 v[66:67], v[66:67], s[6:7], s[6:7] op_sel_hi:[1,0,0]\n\t"
    "v_pk_fma_f32 v[68:69], v[68:69], s[6:7], s[6:7] op_sel_hi:[1,0,0]\n\t"
    "v_pk_fma_f32 v[70:71], v[70:71], s[6:7], s[6:7] op_sel_hi:[1,0,0]\n\t"
    "v_pk_fma_f32 v[72:73], v[72:73], s[6:7], s[6:7] op_sel_hi:[1,0,0]\n\t"
    "v_pk_fma_f32 v[74:75], v[74:75], s[6:7], s[6:7] op_sel_hi:[1,0,0]\n\t"
    "v_pk_fma_f32 v[76:77], v[76:77], s[6:7], s[6:7] op_sel_hi:[1,0,0]\n\t"
    "v_pk_fma_f32 v[78:79], v[78:79], s[6:7], s[6:7] op_sel_hi:[1,0,0]\n\t"
    "v_pk_fma_f32 v[232:233], v[232:233], v[112:113], v[114:115]\n\t"
    "v_pk_fma_f32 v[234:235], v[234:235], v[116:117], v[118:119]\n\t"
    "v_pk_fma_f32 v[236:237], v[236:237], v[120:121], v[122:123]\n\t"
    "v_pk_fma_f32 v[238:239], v[238:239], v[124:125], v[126:127]\n\t"
    "v_pk_fma_f32 v[248:249], v[248:249], v[112:113], v[114:115]\n\t"
    "v_pk_fma_f32 v[250:251], v[250:251], v[116:117], v[118:119]\n\t"
    "v_pk_fma_f32 v[252:253], v[252:253], v[120:121], v[122:123]\n\t"
    "v_pk_fma_f32 v[254:255], v[254:255], v[124:125], v[126:127]\n\t"
    "v_rcp_f32 v64, v64\n\t"
    "v_pk_fma_f32 v[80:81], v[232:233], v[232:233], s[4:5] op_sel_hi:[1,1,0] neg_lo:[1,0,0] neg_hi:[1,0,0]\n\t"
    "v_rcp_f32 v65, v65\n\t"
    "v_pk_fma_f32 v[82:83], v[234:235], v[234:235], s[4:5] op_sel_hi:[1,1,0] neg_lo:[1,0,0] neg_hi:[1,0,0]\n\t"
    "ds_read_b32 v33, v45 offset:20\n\t"
    "ds_read_b128 v[16:19], v42 offset:40960\n\t"
    "ds_read_b128 v[20:23], v42 offset:41984\n\t"
    "v_rcp_f32 v66, v66\n\t"
    "v_pk_fma_f32 v[84:85], v[236:237], v[236:237], s[4:5] op_sel_hi:[1,1,0] neg_lo:[1,0,0] neg_hi:[1,0,0]\n\t"
    "v_rcp_f32 v67, v67\n\t"
    "v_pk_fma_f32 v[86:87], v[238:239], v[238:239], s[4:5] op_sel_hi:[1,1,0] neg_lo:[1,0,0] neg_hi:[1,0,0]\n\t"
    "v_rcp_f32 v68, v68\n\t"
    "v_pk_fma_f32 v[88:89], v[248:249], v[248:249], s[4:5] op_sel_hi:[1,1,0] neg_lo:[1,0,0] neg_hi:[1,0,0]\n\t"
    "v_rcp_f32 v69, v69\n\t"
    "v_pk_fma_f32 v[90:91], v[250:251], v[250:251], s[4:5] op_sel_hi:[1,1,0] neg_lo:[1,0,0] neg_hi:[1,0,0]\n\t"
    "v_rcp_f32 v70, v70\n\t"
    "v_pk_fma_f32 v[92:93], v[252:253], v[252:253], s[4:5] op_sel_hi:[1,1,0] neg_lo:[1,0,0] neg_hi:[1,0,0]\n\t"
    "v_rcp_f32 v71, v71\n\t"
    "v_pk_fma_f32 v[94:95], v[254:255], v[254:255], s[4:5] op_sel_hi:[1,1,0] neg_lo:[1,0,0] neg_hi:[1,0,0]\n\t"
    "v_rcp_f32 v72, v72\n\t"
    "v_pk_mul_f32 v[80:81], v[80:81], v[232:233]\n\t"
    "v_rcp_f32 v73, v73\n\t"
    "v_pk_mul_f32 v[82:83], v[82:83], v[234:235]\n\t"
    "v_rcp_f32 v74, v74\n\t"
    "v_pk_mul_f32 v[84:85], v[84:85], v[236:237]\n\t"
    "s_waitcnt lgkmcnt(0)\n\t"
    "v_cmp_eq_u32 vcc, 0, v33\n\t"
    "v_rcp_f32 v75, v75\n\t"
    "v_pk_mul_f32 v[86:87], v[86:87], v[238:239]\n\t"
    "v_rcp_f32 v76, v76\n\t"
    "s_cbranch_vccnz L_rb5_%=\n\t"
    "L_rbok5_%=:\n\t"
    "buffer_store_dwordx4 v[16:19], v41, s[84:87], s72 offen offset:2176 nt\n\t"
    "v_pk_mul_f32 v[88:89], v[88:89], v[248:249]\n\t"
    "v_rcp_f32 v77, v77\n\t"
    "v_pk_mul_f32 v[90:91], v[90:91], v[250:251]\n\t"
    "v_rcp_f32 v78, v78\n\t"
    "v_pk_mul_f32 v[92:93], v[92:93], v[252:253]\n\t"
    "v_rcp_f32 v79, v79\n\t"
    "v_pk_mul_f32 v[94:95], v[94:95], v[254:255]\n\t"
    "buffer_store_dwordx4 v[20:23], v41, s[84:87], s73 offen offset:2176 nt\n\t"
    "v_exp_f32 v80, v80\n\t"
    "v_pk_mul_f32 v[200:201], v[200:201], v[64:65]\n\t"
    "v_exp_f32 v81, v81\n\t"
    "v_pk_mul_f32 v[202:203], v[202:203], v[66:67]\n\t"
    "v_exp_f32 v82, v82\n\t"
    "v_cvt_pk_bf16_f32 v0, v200, v201\n\t"
    "v_exp_f32 v83, v83\n\t"
    "v_cvt_pk_bf16_f32 v1, v202, v203\n\t"
    "v_exp_f32 v84, v84\n\t"
    "v_pk_mul_f32 v[216:217], v[216:217], v[72:73]\n\t"
    "v_exp_f32 v85, v85\n\t"
    "v_pk_mul_f32 v[218:219], v[218:219], v[74:75]\n\t"
    "v_exp_f32 v86, v86\n\t"
    "v_cvt_pk_bf16_f32 v4, v216, v217\n\t"
    "v_exp_f32 v87, v87\n\t"
    "v_cvt_pk_bf16_f32 v5, v218, v219\n\t"
    "ds_write2st64_b64 v43, v[0:1], v[4:5] offset0:96 offset1:100\n\t"
    "v_exp_f32 v88, v88\n\t"
    "v_pk_mul_f32 v[204:205], v[204:205], v[68:69]\n\t"
    "v_exp_f32 v89, v89\n\t"
    "v_pk_mul_f32 v[206:207], v[206:207], v[70:71]\n\t"
    "v_exp_f32 v90, v90\n\t"
    "v_cvt_pk_bf16_f32 v2, v204, v205\n\t"
    "v_exp_f32 v91, v91\n\t"
    "v_cvt_pk_bf16_f32 v3, v206, v207\n\t"
    "v_exp_f32 v92, v92\n\t"
    "v_pk_mul_f32 v[220:221], v[220:221], v[76:77]\n\t"
    "v_exp_f32 v93, v93\n\t"
    "v_pk_mul_f32 v[222:223], v[222:223], v[78:79]\n\t"
    "v_exp_f32 v94, v94\n\t"
    "v_cvt_pk_bf16_f32 v6, v220, v221\n\t"
    "v_exp_f32 v95, v95\n\t"
    "v_cvt_pk_bf16_f32 v7, v222, v223\n\t"
    "ds_write2st64_b64 v44, v[2:3], v[6:7] offset0:96 offset1:100\n\t"
    "ds_write_b32 v47, v46 offset:24\n\t"
    "v_pk_fma_f32 v[80:81], v[80:81], s[6:7], s[6:7] op_sel_hi:[1,0,0]\n\t"
    "v_pk_fma_f32 v[82:83], v[82:83], s[6:7], s[6:7] op_sel_hi:[1,0,0]\n\t"
    "v_pk_fma_f32 v[84:85], v[84:85], s[6:7], s[6:7] op_sel_hi:[1,0,0]\n\t"
    "v_pk_fma_f32 v[86:87], v[86:87], s[6:7], s[6:7] op_sel_hi:[1,0,0]\n\t"
    "v_pk_fma_f32 v[88:89], v[88:89], s[6:7], s[6:7] op_sel_hi:[1,0,0]\n\t"
    "v_pk_fma_f32 v[90:91], v[90:91], s[6:7], s[6:7] op_sel_hi:[1,0,0]\n\t"
    "v_pk_fma_f32 v[92:93], v[92:93], s[6:7], s[6:7] op_sel_hi:[1,0,0]\n\t"
    "v_pk_fma_f32 v[94:95], v[94:95], s[6:7], s[6:7] op_sel_hi:[1,0,0]\n\t"
    "v_rcp_f32 v80, v80\n\t"
    "v_rcp_f32 v81, v81\n\t"
    "v_rcp_f32 v82, v82\n\t"
    "ds_read_b32 v33, v45 offset:24\n\t"
    "ds_read_b128 v[16:19], v42 offset:49152\n\t"
    "ds_read_b128 v[20:23], v42 offset:50176\n\t"
    "v_rcp_f32 v83, v83\n\t"
    "v_rcp_f32 v84, v84\n\t"
    "v_rcp_f32 v85, v85\n\t"
    "v_rcp_f32 v86, v86\n\t"
    "v_rcp_f32 v87, v87\n\t"
    "v_rcp_f32 v88, v88\n\t"
    "v_rcp_f32 v89, v89\n\t"
    "v_rcp_f32 v90, v90\n\t"
    "s_waitcnt lgkmcnt(0)\n\t"
    "v_cmp_eq_u32 vcc, 0, v33\n\t"
    "v_rcp_f32 v91, v91\n\t"
    "v_rcp_f32 v92, v92\n\t"
    "v_rcp_f32 v93, v93\n\t"
    "s_cbranch_vccnz L_rb6_%=\n\t"
    "L_rbok6_%=:\n\t"
    "buffer_store_dwordx4 v[16:19], v41, s[84:87], s74 offen offset:128 nt\n\t"
    "v_rcp_f32 v94, v94\n\t"
    "v_rcp_f32 v95, v95\n\t"
    "buffer_store_dwordx4 v[20:23], v41, s[84:87], s75 offen offset:128 nt\n\t"
    "s_nop 0\n\t"
    "v_pk_mul_f32 v[232:233], v[232:233], v[80:81]\n\t"
    "v_pk_mul_f32 v[234:235], v[234:235], v[82:83]\n\t"
    "v_cvt_pk_bf16_f32 v8, v232, v233\n\t"
    "v_cvt_pk_bf16_f32 v9, v234, v235\n\t"
    "v_pk_mul_f32 v[248:249], v[248:249], v[88:89]\n\t"
    "v_pk_mul_f32 v[250:251], v[250:251], v[90:91]\n\t"
    "v_cvt_pk_bf16_f32 v12, v248, v249\n\t"
    "v_cvt_pk_bf16_f32 v13, v250, v251\n\t"
    "ds_write2st64_b64 v43, v[8:9], v[12:13] offset0:112 offset1:116\n\t"
    "v_pk_mul_f32 v[236:237], v[236:237], v[84:85]\n\t"
    "v_pk_mul_f32 v[238:239], v[238:239], v[86:87]\n\t"
    "v_cvt_pk_bf16_f32 v10, v236, v237\n\t"
    "v_cvt_pk_bf16_f32 v11, v238, v239\n\t"
    "v_pk_mul_f32 v[252:253], v[252:253], v[92:93]\n\t"
    "v_pk_mul_f32 v[254:255], v[254:255], v[94:95]\n\t"
    "v_cvt_pk_bf16_f32 v14, v252, v253\n\t"
    "v_cvt_pk_bf16_f32 v15, v254, v255\n\t"
    "ds_write2st64_b64 v44, v[10:11], v[14:15] offset0:112 offset1:116\n\t"
    "ds_write_b32 v47, v46 offset:28\n\t"
    "ds_read_b32 v33, v45 offset:28\n\t"
    "ds_read_b128 v[16:19], v42 offset:57344\n\t"
    "ds_read_b128 v[20:23], v42 offset:58368\n\t"
    "s_waitcnt lgkmcnt(0)\n\t"
    "v_cmp_eq_u32 vcc, 0, v33\n\t"
    "s_cbranch_vccnz L_rb7_%=\n\t"
    "L_rbok7_%=:\n\t"
    "buffer_store_dwordx4 v[16:19], v41, s[84:87], s74 offen offset:2176 nt\n\t"
    "buffer_store_dwordx4 v[20:23], v41, s[84:87], s75 offen offset:2176 nt\n\t"
    "s_branch L_done_%=\n\t"
    "L_flagwait_%=:\n\t"
    "s_mov_b32 s4, 0\n\t"
    "L_fw1_%=:\n\t"
    "s_sleep 2\n\t"
    "buffer_load_dwordx2 v[50:51], v31, s[60:63], 0 offen sc1\n\t"
    "s_waitcnt vmcnt(0)\n\t"
    "v_cmp_ne_u64 vcc, v[50:51], v[48:49]\n\t"
    "s_add_u32 s4, s4, 1\n\t"
    "s_cmp_ge_u32 s4, 0x100000\n\t"
    "s_cbranch_scc1 L_flagok_%=\n\t"
    "s_cbranch_vccnz L_fw1_%=\n\t"
    "s_branch L_flagok_%=\n\t"
    "L_gchkA_%=:\n\t"
    "v_max3_u32 v33, v96, v97, v98\n\t"
    "v_max3_u32 v33, v33, v99, v64\n\t"
    "v_max3_u32 v33, v33, v65, v66\n\t"
    "v_max3_u32 v33, v33, v67, v68\n\t"
    "v_max3_u32 v33, v33, v69, v70\n\t"
    "v_max3_u32 v33, v33, v71, v72\n\t"
    "v_max3_u32 v33, v33, v73, v74\n\t"
    "v_max3_u32 v33, v33, v75, v112\n\t"
    "v_max3_u32 v33, v33, v113, v114\n\t"
    "v_max3_u32 v33, v33, v115, v116\n\t"
    "v_max3_u32 v33, v33, v117, v118\n\t"
    "v_max3_u32 v33, v33, v119, v120\n\t"
    "v_max3_u32 v33, v33, v121, v122\n\t"
    "v_max3_u32 v33, v33, v123, v124\n\t"
    "v_max3_u32 v33, v33, v125, v126\n\t"
    "v_max_u32 v33, v33, v127\n\t"
    "v_cmp_eq_u32 vcc, -1, v33\n\t"
    "s_add_u32 s66, s66, 1\n\t"
    "s_cmp_ge_u32 s66, 0x100000\n\t"
    "s_cbranch_scc1 L_gokA_%=\n\t"
    "s_cbranch_vccz L_gokA_%=\n\t"
    "s_mov_b32 exec_lo, 0xfff0fff0\n\t"
    "s_mov_b32 exec_hi, 0xfff0fff0\n\t"
    "buffer_load_dwordx4 v[96:99], v38, s[60:63], s8 offen offset:0 sc1\n\t"
    "s_mov_b64 exec, -1\n\t"
    "buffer_load_dwordx4 v[64:67], v38, s[60:63], s9 offen offset:0 sc1\n\t"
    "buffer_load_dwordx4 v[68:71], v38, s[60:63], s10 offen offset:0 sc1\n\t"
    "buffer_load_dwordx4 v[72:75], v38, s[60:63], s11 offen offset:0 sc1\n\t"
    "buffer_load_dwordx4 v[112:115], v38, s[60:63], s12 offen offset:0 sc1\n\t"
    "buffer_load_dwordx4 v[116:119], v38, s[60:63], s13 offen offset:0 sc1\n\t"
    "buffer_load_dwordx4 v[120:123], v38, s[60:63], s14 offen offset:0 sc1\n\t"
    "buffer_load_dwordx4 v[124:127], v38, s[60:63], s15 offen offset:0 sc1\n\t"
    "s_waitcnt vmcnt(0)\n\t"
    "s_branch L_gsumA_%=\n\t"
    "L_rb0_%=:\n\t"
    "s_add_u32 s67, s67, 1\n\t"
    "s_cmp_ge_u32 s67, 0x1000000\n\t"
    "s_cbranch_scc1 L_rbok0_%=\n\t"
    "ds_read_b32 v33, v45 offset:0\n\t"
    "ds_read_b128 v[16:19], v42 offset:0\n\t"
    "ds_read_b128 v[20:23], v42 offset:1024\n\t"
    "s_waitcnt lgkmcnt(0)\n\t"
    "v_cmp_eq_u32 vcc, 0, v33\n\t"
    "s_cbranch_vccnz L_rb0_%=\n\t"
    "s_branch L_rbok0_%=\n\t"
    "L_rb1_%=:\n\t"
    "s_add_u32 s67, s67, 1\n\t"
    "s_cmp_ge_u32 s67, 0x1000000\n\t"
    "s_cbranch_scc1 L_rbok1_%=\n\t"
    "ds_read_b32 v33, v45 offset:4\n\t"
    "ds_read_b128 v[16:19], v42 offset:8192\n\t"
    "ds_read_b128 v[20:23], v42 offset:9216\n\t"
    "s_waitcnt lgkmcnt(0)\n\t"
    "v_cmp_eq_u32 vcc, 0, v33\n\t"
    "s_cbranch_vccnz L_rb1_%=\n\t"
    "s_branch L_rbok1_%=\n\t"
    "L_rb2_%=:\n\t"
    "s_add_u32 s67, s67, 1\n\t"
    "s_cmp_ge_u32 s67, 0x1000000\n\t"
    "s_cbranch_scc1 L_rbok2_%=\n\t"
    "ds_read_b32 v33, v45 offset:8\n\t"
    "ds_read_b128 v[16:19], v42 offset:16384\n\t"
    "ds_read_b128 v[20:23], v42 offset:17408\n\t"
    "s_waitcnt lgkmcnt(0)\n\t"
    "v_cmp_eq_u32 vcc, 0, v33\n\t"
    "s_cbranch_vccnz L_rb2_%=\n\t"
    "s_branch L_rbok2_%=\n\t"
    "L_gchkB_%=:\n\t"
    "v_max3_u32 v33, v24, v25, v26\n\t"
    "v_max3_u32 v33, v33, v27, v112\n\t"
    "v_max3_u32 v33, v33, v113, v114\n\t"
    "v_max3_u32 v33, v33, v115, v116\n\t"
    "v_max3_u32 v33, v33, v117, v118\n\t"
    "v_max3_u32 v33, v33, v119, v120\n\t"
    "v_max3_u32 v33, v33, v121, v122\n\t"
    "v_max3_u32 v33, v33, v123, v124\n\t"
    "v_max3_u32 v33, v33, v125, v126\n\t"
    "v_max3_u32 v33, v33, v127, v34\n\t"
    "v_max3_u32 v33, v33, v35, v36\n\t"
    "v_max3_u32 v33, v33, v37, v56\n\t"
    "v_max3_u32 v33, v33, v57, v58\n\t"
    "v_max3_u32 v33, v33, v59, v52\n\t"
    "v_max3_u32 v33, v33, v53, v54\n\t"
    "v_max_u32 v33, v33, v55\n\t"
    "v_cmp_eq_u32 vcc, -1, v33\n\t"
    "s_add_u32 s66, s66, 1\n\t"
    "s_cmp_ge_u32 s66, 0x100000\n\t"
    "s_cbranch_scc1 L_gokB_%=\n\t"
    "s_cbranch_vccz L_gokB_%=\n\t"
    "s_mov_b32 exec_lo, 0xfff0fff0\n\t"
    "s_mov_b32 exec_hi, 0xfff0fff0\n\t"
    "buffer_load_dwordx4 v[24:27], v38, s[60:63], s8 offen offset:512 sc1\n\t"
    "s_mov_b64 exec, -1\n\t"
    "buffer_load_dwordx4 v[112:115], v38, s[60:63], s9 offen offset:512 sc1\n\t"
    "buffer_load_dwordx4 v[116:119], v38, s[60:63], s10 offen offset:512 sc1\n\t"
    "buffer_load_dwordx4 v[120:123], v38, s[60:63], s11 offen offset:512 sc1\n\t"
    "buffer_load_dwordx4 v[124:127], v38, s[60:63], s12 offen offset:512 sc1\n\t"
    "buffer_load_dwordx4 v[34:37], v38, s[60:63], s13 offen offset:512 sc1\n\t"
    "buffer_load_dwordx4 v[56:59], v38, s[60:63], s14 offen offset:512 sc1\n\t"
    "buffer_load_dwordx4 v[52:55], v38, s[60:63], s15 offen offset:512 sc1\n\t"
    "s_waitcnt vmcnt(0)\n\t"
    "s_branch L_gsumB_%=\n\t"
    "L_rb3_%=:\n\t"
    "s_add_u32 s67, s67, 1\n\t"
    "s_cmp_ge_u32 s67, 0x1000000\n\t"
    "s_cbranch_scc1 L_rbok3_%=\n\t"
    "ds_read_b32 v33, v45 offset:12\n\t"
    "ds_read_b128 v[16:19], v42 offset:24576\n\t"
    "ds_read_b128 v[20:23], v42 offset:25600\n\t"
    "s_waitcnt lgkmcnt(0)\n\t"
    "v_cmp_eq_u32 vcc, 0, v33\n\t"
    "s_cbranch_vccnz L_rb3_%=\n\t"
    "s_branch L_rbok3_%=\n\t"
    "L_rb4_%=:\n\t"
    "s_add_u32 s67, s67, 1\n\t"
    "s_cmp_ge_u32 s67, 0x1000000\n\t"
    "s_cbranch_scc1 L_rbok4_%=\n\t"
    "ds_read_b32 v33, v45 offset:16\n\t"
    "ds_read_b128 v[16:19], v42 offset:32768\n\t"
    "ds_read_b128 v[20:23], v42 offset:33792\n\t"
    "s_waitcnt lgkmcnt(0)\n\t"
    "v_cmp_eq_u32 vcc, 0, v33\n\t"
    "s_cbranch_vccnz L_rb4_%=\n\t"
    "s_branch L_rbok4_%=\n\t"
    "L_rb5_%=:\n\t"
    "s_add_u32 s67, s67, 1\n\t"
    "s_cmp_ge_u32 s67, 0x1000000\n\t"
    "s_cbranch_scc1 L_rbok5_%=\n\t"
    "ds_read_b32 v33, v45 offset:20\n\t"
    "ds_read_b128 v[16:19], v42 offset:40960\n\t"
    "ds_read_b128 v[20:23], v42 offset:41984\n\t"
    "s_waitcnt lgkmcnt(0)\n\t"
    "v_cmp_eq_u32 vcc, 0, v33\n\t"
    "s_cbranch_vccnz L_rb5_%=\n\t"
    "s_branch L_rbok5_%=\n\t"
    "L_rb6_%=:\n\t"
    "s_add_u32 s67, s67, 1\n\t"
    "s_cmp_ge_u32 s67, 0x1000000\n\t"
    "s_cbranch_scc1 L_rbok6_%=\n\t"
    "ds_read_b32 v33, v45 offset:24\n\t"
    "ds_read_b128 v[16:19], v42 offset:49152\n\t"
    "ds_read_b128 v[20:23], v42 offset:50176\n\t"
    "s_waitcnt lgkmcnt(0)\n\t"
    "v_cmp_eq_u32 vcc, 0, v33\n\t"
    "s_cbranch_vccnz L_rb6_%=\n\t"
    "s_branch L_rbok6_%=\n\t"
    "L_rb7_%=:\n\t"
    "s_add_u32 s67, s67, 1\n\t"
    "s_cmp_ge_u32 s67, 0x1000000\n\t"
    "s_cbranch_scc1 L_rbok7_%=\n\t"
    "ds_read_b32 v33, v45 offset:28\n\t"
    "ds_read_b128 v[16:19], v42 offset:57344\n\t"
    "ds_read_b128 v[20:23], v42 offset:58368\n\t"
    "s_waitcnt lgkmcnt(0)\n\t"
    "v_cmp_eq_u32 vcc, 0, v33\n\t"
    "s_cbranch_vccnz L_rb7_%=\n\t"
    "s_branch L_rbok7_%=\n\t"
    "L_done_%=:\n\t"
    : "+{v0}"(tid), "+{s2}"(bid), "+{s[0:1]}"(kp)    : "i"(0), "{v63}"((unsigned)(size_t)(LAS unsigned char*)lds)
    : "memory", "vcc", "scc", "s3", "s4", "s5", "s6", "s7", "s8", "s9", "s10", "s11", "s12", "s13", "s14", "s15", "s16", "s17", "s18", "s19", "s20", "s21", "s22", "s23", "s24", "s25", "s26", "s27", "s28", "s29", "s30", "s31", "s33", "s34", "s35", "s36", "s37", "s38", "s39", "s40", "s41", "s42", "s43", "s44", "s45", "s46", "s47", "s48", "s49", "s50", "s51", "s52", "s53", "s54", "s55", "s56", "s57", "s58", "s59", "s60", "s61", "s62", "s63", "s64", "s65", "s66", "s67", "s68", "s69", "s70", "s71", "s72", "s73", "s74", "s75", "s76", "s77", "s78", "s79", "s80", "s81", "s82", "s83", "s84", "s85", "s86", "s87",
      "v1", "v2", "v3", "v4", "v5", "v6", "v7", "v8", "v9", "v10", "v11", "v12", "v13", "v14", "v15", "v16", "v17", "v18", "v19", "v20", "v21", "v22", "v23", "v24", "v25", "v26", "v27", "v28", "v29", "v30", "v31", "v32", "v33", "v34", "v35", "v36", "v37", "v38", "v39", "v40", "v41", "v42", "v43", "v44", "v45", "v46", "v47", "v48", "v49", "v50", "v51", "v52", "v53", "v54", "v55", "v56", "v57", "v58", "v59", "v60", "v61", "v62", "v64", "v65", "v66", "v67", "v68", "v69", "v70", "v71", "v72", "v73", "v74", "v75", "v76", "v77", "v78", "v79", "v80", "v81", "v82", "v83", "v84", "v85", "v86", "v87", "v88", "v89", "v90", "v91", "v92", "v93", "v94", "v95", "v96", "v97", "v98", "v99", "v100", "v101", "v102", "v103", "v104", "v105", "v106", "v107", "v108", "v109", "v110", "v111", "v112", "v113", "v114", "v115", "v116", "v117", "v118", "v119", "v120", "v121", "v122", "v123", "v124", "v125", "v126", "v127", "v128", "v129", "v130", "v131", "v132", "v133", "v134", "v135", "v136", "v137", "v138", "v139", "v140", "v141", "v142", "v143", "v144", "v145", "v146", "v147", "v148", "v149", "v150", "v151", "v152", "v153", "v154", "v155", "v156", "v157", "v158", "v159", "v160", "v161", "v162", "v163", "v164", "v165", "v166", "v167", "v168", "v169", "v170", "v171", "v172", "v173", "v174", "v175", "v176", "v177", "v178", "v179", "v180", "v181", "v182", "v183", "v184", "v185", "v186", "v187", "v188", "v189", "v190", "v191", "v192", "v193", "v194", "v195", "v196", "v197", "v198", "v199", "v200", "v201", "v202", "v203", "v204", "v205", "v206", "v207", "v208", "v209", "v210", "v211", "v212", "v213", "v214", "v215", "v216", "v217", "v218", "v219", "v220", "v221", "v222", "v223", "v224", "v225", "v226", "v227", "v228", "v229", "v230", "v231", "v232", "v233", "v234", "v235", "v236", "v237", "v238", "v239", "v240", "v241", "v242", "v243", "v244", "v245", "v246", "v247", "v248", "v249", "v250", "v251", "v252", "v253", "v254", "v255",
      "a0", "a1", "a2", "a3", "a4", "a5", "a6", "a7", "a8", "a9", "a10", "a11", "a12", "a13", "a14", "a15", "a16", "a17", "a18", "a19", "a20", "a21", "a22", "a23", "a24", "a25", "a26", "a27", "a28", "a29", "a30", "a31", "a32", "a33", "a34", "a35", "a36", "a37", "a38", "a39", "a40", "a41", "a42", "a43", "a44", "a45", "a46", "a47", "a48", "a49", "a50", "a51", "a52", "a53", "a54", "a55", "a56", "a57", "a58", "a59", "a60", "a61", "a62", "a63", "a64", "a65", "a66", "a67", "a68", "a69", "a70", "a71", "a72", "a73", "a74", "a75", "a76", "a77", "a78", "a79", "a80", "a81", "a82", "a83", "a84", "a85", "a86", "a87", "a88", "a89", "a90", "a91", "a92", "a93", "a94", "a95", "a96", "a97", "a98", "a99", "a100", "a101", "a102", "a103", "a104", "a105", "a106", "a107", "a108", "a109", "a110", "a111", "a112", "a113", "a114", "a115", "a116", "a117", "a118", "a119", "a120", "a121", "a122", "a123", "a124", "a125", "a126", "a127", "a128", "a129", "a130", "a131", "a132", "a133", "a134", "a135", "a136", "a137", "a138", "a139", "a140", "a141", "a142", "a143", "a144", "a145", "a146", "a147", "a148", "a149", "a150", "a151", "a152", "a153", "a154", "a155", "a156", "a157", "a158", "a159", "a160", "a161", "a162", "a163", "a164", "a165", "a166", "a167", "a168", "a169", "a170", "a171", "a172", "a173", "a174", "a175", "a176", "a177", "a178", "a179", "a180", "a181", "a182", "a183", "a184", "a185", "a186", "a187", "a188", "a189", "a190", "a191", "a192", "a193", "a194", "a195", "a196", "a197", "a198", "a199", "a200", "a201", "a202", "a203", "a204", "a205", "a206", "a207", "a208", "a209", "a210", "a211", "a212", "a213", "a214", "a215", "a216", "a217", "a218", "a219", "a220", "a221", "a222", "a223", "a224", "a225", "a226", "a227", "a228", "a229", "a230", "a231", "a232", "a233", "a234", "a235", "a236", "a237", "a238", "a239", "a240", "a241", "a242", "a243", "a244", "a245", "a246", "a247", "a248", "a249", "a250", "a251", "a252", "a253", "a254", "a255");
  asm volatile("L_kernel_end_marker:");
}

extern "C" void kernel_launch(void* const* d_in, const int* in_sizes, int n_in, void* d_out, int out_size, void* d_ws, size_t ws_size, hipStream_t stream) {
  Args a;
  for (int i = 0; i < 64; ++i) {
    a.l[i].p[0] = d_in[0]; a.l[i].p[1] = d_in[1]; a.l[i].p[2] = d_in[2]; a.l[i].p[3] = d_in[3]; a.l[i].p[4] = d_out; a.l[i].p[5] = d_ws;
    a.l[i].zero[0] = 0; a.l[i].zero[1] = 0; }
  conv_stem_kernel<<<256, 256, 0, stream>>>(a);
}
```
